```python
import torch

def _softmax(x, axis=-1):
    return torch.softmax(x, dim=axis)
def _gelu(x):
    return torch.nn.functional.gelu(x, approximate="tanh")
def _silu(x):
    return torch.nn.functional.silu(x)
def _sigmoid(x):
    return torch.sigmoid(x)
def _layernorm(x, axis=-1, eps=1e-5):
    m = x.mean(dim=axis, keepdim=True)
    v = x.var(dim=axis, keepdim=True, unbiased=False)
    return (x - m) / torch.sqrt(v + eps)
def _scatter_add(tgt, idx, src, axis=0):
    return tgt.clone().index_add_(axis, idx, src)
def _segment_sum(src, seg, n):
    out = torch.zeros((n,) + src.shape[1:], dtype=src.dtype)
    return out.index_add_(0, seg, src)
def _pad(x, pw):
    flat = tuple(v for pair in reversed(pw) for v in pair)
    return torch.nn.functional.pad(x, flat)

kernel_name = 'standalone__spectral_fnet__prompt'

def setup_inputs():

    _w = (lambda a: (lambda t: t.to(torch.bfloat16) if t.is_floating_point() else t)(torch.as_tensor(a)))
    B, S, D = 1, 2048, 448
    ns = torch.arange(S)[:, None] * torch.arange(S)[None, :]
    nd = torch.arange(D)[:, None] * torch.arange(D)[None, :]
    Ws_re = torch.cos(-2.0 * torch.pi * ns / S).to(float)
    Ws_im = torch.sin(-2.0 * torch.pi * ns / S).to(float)
    Wd_re = torch.cos(-2.0 * torch.pi * nd / D).to(float)
    Wd_im = torch.sin(-2.0 * torch.pi * nd / D).to(float)
    return {
        "x": _w((lambda *s: torch.randn(*s, dtype=torch.bfloat16) * 0.05)(B, S, D)),
        "dft_seq_re": _w(Ws_re),
        "dft_seq_im": _w(Ws_im),
        "dft_hid_re": _w(Wd_re),
        "dft_hid_im": _w(Wd_im),
    }

def reference(x, dft_seq_re, dft_seq_im, dft_hid_re, dft_hid_im):
    a = torch.einsum("bsd,ed->bse", x, dft_hid_re)
    b = torch.einsum("bsd,ed->bse", x, dft_hid_im)
    y = torch.einsum("ts,bse->bte", dft_seq_re, a) - torch.einsum("ts,bse->bte", dft_seq_im, b)
    return y
```

```cpp
#include <hip/hip_runtime.h>
#include <hip/hip_bf16.h>
typedef short bf16x8 __attribute__((ext_vector_type(8)));
typedef float f32x4 __attribute__((ext_vector_type(4)));
typedef unsigned short u16;
constexpr int S = 2048, D = 448;

__device__ __forceinline__ u16 f2bf(float f) { return __builtin_bit_cast(u16, (__bf16)f); }

__global__ void k1(const u16* x, const u16* wre, const u16* wim, u16* abT) {
  int wave = (blockIdx.x * blockDim.x + threadIdx.x) >> 6, lane = threadIdx.x & 63;
  int et = wave % 56, st = wave / 56;
  const u16* w = et < 28 ? wre + (size_t)(et * 16) * D : wim + (size_t)((et - 28) * 16) * D;
  f32x4 acc = {0, 0, 0, 0};
  for (int k = 0; k < D; k += 32) {
    bf16x8 a = *(const bf16x8*)(x + (size_t)(st * 16 + (lane & 15)) * D + k + 8 * (lane >> 4));
    bf16x8 b = *(const bf16x8*)(w + (size_t)(lane & 15) * D + k + 8 * (lane >> 4));
    acc = __builtin_amdgcn_mfma_f32_16x16x32_bf16(a, b, acc, 0, 0, 0);
  }
  int e = et * 16 + (lane & 15);
  float sg = et < 28 ? 1.f : -1.f;
  int eo = e % 448, half = e / 448;
  for (int r = 0; r < 4; ++r) abT[(size_t)eo * 4096 + half * 2048 + st * 16 + 4 * (lane >> 4) + r] = f2bf(sg * acc[r]);
}
__global__ void k2(const u16* sre, const u16* sim, const u16* abT, u16* y) {
  int wave = (blockIdx.x * blockDim.x + threadIdx.x) >> 6, lane = threadIdx.x & 63;
  int et = wave % 28, tt = wave / 28;
  f32x4 acc = {0, 0, 0, 0};
  for (int k = 0; k < 4096; k += 32) {
    const u16* w = k < 2048 ? sre : sim;
    bf16x8 b = *(const bf16x8*)(w + (size_t)(tt * 16 + (lane & 15)) * S + (k & 2047) + 8 * (lane >> 4));
    bf16x8 a = *(const bf16x8*)(abT + (size_t)(et * 16 + (lane & 15)) * 4096 + k + 8 * (lane >> 4));
    acc = __builtin_amdgcn_mfma_f32_16x16x32_bf16(a, b, acc, 0, 0, 0);
  }
  int t = tt * 16 + (lane & 15);
  for (int r = 0; r < 4; ++r) y[(size_t)t * D + et * 16 + 4 * (lane >> 4) + r] = f2bf(acc[r]);
}

extern "C" void kernel_launch(void* const* d_in, const int* in_sizes, int n_in, void* d_out, int out_size, void* d_ws, size_t ws_size,
                              hipStream_t stream) {
  const u16* x = (const u16*)d_in[0];
  u16* abT = (u16*)d_ws;
  k1<<<56 * 128 / 4, 256, 0, stream>>>(x, (const u16*)d_in[3], (const u16*)d_in[4], abT);
  k2<<<28 * 128 / 4, 256, 0, stream>>>((const u16*)d_in[1], (const u16*)d_in[2], abT, (u16*)d_out);
}
```

```cpp
#include <hip/hip_runtime.h>
#include <hip/hip_bf16.h>
typedef unsigned short u16;
typedef short s16x4 __attribute__((ext_vector_type(4)));
typedef short bf16x8 __attribute__((ext_vector_type(8)));
typedef float f32x4 __attribute__((ext_vector_type(4)));
typedef float f32x16 __attribute__((ext_vector_type(16)));
typedef unsigned v4u __attribute__((ext_vector_type(4)));
typedef unsigned v2u __attribute__((ext_vector_type(2)));
typedef __bf16 bf2 __attribute__((ext_vector_type(2)));
typedef float f32x2 __attribute__((ext_vector_type(2)));
#define GAS __attribute__((address_space(1)))
#define LAS __attribute__((address_space(3)))
#define DI __device__ __forceinline__

constexpr int SEQ = 2048, DH = 448;
constexpr int SLOT = 32768, NSLOT = 5;
constexpr int PQ_BYTES = 16 * 8 * 2 * 2 * 14 * 1024;
constexpr int RSRC_W3 = 0x00020000;

#define STAMP(i)

DI unsigned pk2(float a, float b) { bf2 r = __builtin_convertvector(f32x2{a, b}, bf2); return __builtin_bit_cast(unsigned, r); }
DI void dma16(__amdgpu_buffer_rsrc_t rs, LAS unsigned char* dst, int voff, int soff) {
  __builtin_amdgcn_raw_ptr_buffer_load_lds(rs, (LAS void*)dst, 16, voff, soff, 0, 0);
}
DI s16x4 trd(LAS unsigned char* p) { return __builtin_amdgcn_ds_read_tr16_b64_v4i16((LAS s16x4*)p); }
#define BAR() __builtin_amdgcn_s_barrier()
#define WAITV(n) asm volatile("s_waitcnt vmcnt(" #n ")" ::: "memory")
#define WAITL0() asm volatile("s_waitcnt lgkmcnt(0)" ::: "memory")
#define SB() __builtin_amdgcn_sched_barrier(0)

template <bool WIDE> struct Frags { bf16x8 w[2]; bf16x8 x[WIDE ? 2 : 1]; };

template <bool WIDE>
DI void loadFrags(Frags<WIDE>& f, LAS unsigned char* wA, LAS unsigned char* x0, LAS unsigned char* x1, int ks) {
  f.w[0] = *(LAS bf16x8*)(wA);
  f.w[1] = *(LAS bf16x8*)(wA + 4096);
  constexpr int RP = WIDE ? 256 : 128;
  {
    s16x4 a = trd(x0 + (16 * ks) * RP), b = trd(x0 + (16 * ks + 4) * RP);
    f.x[0] = __builtin_shufflevector(a, b, 0, 1, 2, 3, 4, 5, 6, 7);
  }
  if constexpr (WIDE) {
    s16x4 a = trd(x1 + (16 * ks) * RP), b = trd(x1 + (16 * ks + 4) * RP);
    f.x[1] = __builtin_shufflevector(a, b, 0, 1, 2, 3, 4, 5, 6, 7);
  }
}
template <bool WIDE> DI void mma(f32x16 (&acc)[WIDE ? 2 : 1][2], const Frags<WIDE>& f) {
#pragma unroll
  for (int dd = 0; dd < (WIDE ? 2 : 1); ++dd)
#pragma unroll
    for (int tt = 0; tt < 2; ++tt) acc[dd][tt] = __builtin_amdgcn_mfma_f32_32x32x16_bf16(f.x[dd], f.w[tt], acc[dd][tt], 0, 0, 0);
}

template <bool WIDE>
DI void phaseA_compute(LAS unsigned char* L, int cw, int lane, void* ws, int panel, int part, int kh, int j) {
  const int wt = cw & 1, wd = cw >> 1, h = lane >> 5, r32 = lane & 31;
  const int f = (lane >> 1) & 7;
  int wo[4];
#pragma unroll
  for (int ks = 0; ks < 4; ++ks) wo[ks] = (wt * 64 + r32) * 128 + (((2 * ks + h) ^ f) << 4);
  const int blk = (lane >> 4) & 1, q = (lane & 15) >> 2, p = lane & 3;
  int xo0, xo1;
  if constexpr (WIDE) {
    const int ch = wd * 8 + 2 * blk + (p >> 1);
    xo0 = 16384 + (8 * h + q) * 256 + ((ch ^ (q << 2)) << 4) + 8 * (p & 1);
    xo1 = xo0 ^ 64;
  } else {
    const int ch = wd * 4 + 2 * blk + (p >> 1);
    xo0 = 16384 + (8 * h + q) * 128 + ((ch ^ (((q >> 1) & 1) << 2)) << 4) + 8 * (p & 1);
    xo1 = xo0;
  }
  f32x16 acc[WIDE ? 2 : 1][2];
#pragma unroll
  for (int dd = 0; dd < (WIDE ? 2 : 1); ++dd)
#pragma unroll
    for (int tt = 0; tt < 2; ++tt)
#pragma unroll
      for (int i = 0; i < 16; ++i) acc[dd][tt][i] = 0.f;
  Frags<WIDE> F0, F1;
  int so = 0;
  BAR();
  STAMP(1);
  loadFrags<WIDE>(F0, L + wo[0], L + xo0, L + xo1, 0);
  for (int s = 0; s < 16; ++s) {
    LAS unsigned char* B = L + so;
    loadFrags<WIDE>(F1, B + wo[1], B + xo0, B + xo1, 1); SB();
    mma<WIDE>(acc, F0); SB();
    loadFrags<WIDE>(F0, B + wo[2], B + xo0, B + xo1, 2); SB();
    mma<WIDE>(acc, F1); SB();
    loadFrags<WIDE>(F1, B + wo[3], B + xo0, B + xo1, 3); SB();
    mma<WIDE>(acc, F0); SB();
    so = (so == (NSLOT - 1) * SLOT) ? 0 : so + SLOT;
    B = L + so;
    WAITL0(); BAR();
    SB();
    if (s < 15) loadFrags<WIDE>(F0, B + wo[0], B + xo0, B + xo1, 0);
    SB();
    mma<WIDE>(acc, F1); SB();
  }
  STAMP(2);
  const __amdgpu_buffer_rsrc_t rs = __builtin_amdgcn_make_buffer_rsrc(ws, 0, PQ_BYTES, RSRC_W3);
  const float sg = part ? -1.f : 1.f;
#pragma unroll
  for (int dd = 0; dd < (WIDE ? 2 : 1); ++dd)
#pragma unroll
    for (int tt = 0; tt < 2; ++tt) {
      const int kap = WIDE ? 4 * j + 2 * wd + dd : 12 + wd;
      const int tau = wt * 4 + tt * 2 + (r32 >> 4);
      const int base = ((((panel * 8 + tau) * 2 + part) * 2 + kh) * 14 + kap) * 1024 + (h * 16 + (lane & 15)) * 16;
#pragma unroll
      for (int u = 0; u < 2; ++u) {
        v4u o;
#pragma unroll
        for (int i = 0; i < 4; ++i) o[i] = pk2(sg * acc[dd][tt][8 * u + 2 * i], sg * acc[dd][tt][8 * u + 2 * i + 1]);
        __builtin_amdgcn_raw_buffer_store_b128(o, rs, base + u * 512, 0, 0);
      }
    }
  STAMP(3);
}

template <bool WIDE>
DI void phaseA_load(LAS unsigned char* L, int lw, int lane, const u16* W, const u16* x, void* ws, int panel, int kh, int j) {
  const __amdgpu_buffer_rsrc_t rw = __builtin_amdgcn_make_buffer_rsrc((void*)W, 0, SEQ * SEQ * 2, RSRC_W3);
  const __amdgpu_buffer_rsrc_t rx = __builtin_amdgcn_make_buffer_rsrc((void*)x, 0, SEQ * DH * 2, RSRC_W3);
  const int vw = (lane >> 3) * 4096 + (((lane & 7) ^ ((4 * (lw & 1) + (lane >> 4)) & 7)) << 4);
  const int vx = WIDE ? (lane >> 4) * 896 + (((lane & 15) ^ ((lane >> 4) << 2)) << 4) : (lane >> 3) * 896 + (((lane & 7) ^ (((lane >> 4) & 1) << 2)) << 4);
  int sw = (panel * 128 + 8 * lw) * 4096 + kh * 2048;
  int sx = (kh * 1024 + (WIDE ? 4 : 8) * lw) * 896 + j * 256;
  auto issue = [&](int so) {
    LAS unsigned char* B = L + so + lw * 1024;
#pragma unroll
    for (int q = 0; q < 4; ++q) dma16(rw, B + q * 4096, vw, sw + q * 32 * 4096);
#pragma unroll
    for (int q = 0; q < (WIDE ? 4 : 2); ++q) dma16(rx, B + 16384 + q * 4096, vx, sx + q * (WIDE ? 16 : 32) * 896);
    sw += 128; sx += 64 * 896;
  };
  for (int s = 0; s < 4; ++s) issue(s * SLOT);
  STAMP(1);
  int so = 4 * SLOT;
  for (int s = 0; s < 12; ++s) {
    if constexpr (WIDE) WAITV(24); else WAITV(18);
    BAR();
    issue(so);
    so = (so == (NSLOT - 1) * SLOT) ? 0 : so + SLOT;
  }
  if constexpr (WIDE) WAITV(24); else WAITV(18);
  BAR();
  if constexpr (WIDE) WAITV(16); else WAITV(12);
  BAR();
  if constexpr (WIDE) WAITV(8); else WAITV(6);
  BAR();
  WAITV(0);
  STAMP(2);
  BAR();
  BAR();
}

__global__ __launch_bounds__(512, 2) void kA(const u16* __restrict__ x, const u16* __restrict__ sre, const u16* __restrict__ sim, void* ws) {
  __shared__ __attribute__((aligned(16))) unsigned char smem[NSLOT * SLOT];
  LAS unsigned char* L = (LAS unsigned char*)smem;
  const int lane = threadIdx.x & 63, w = __builtin_amdgcn_readfirstlane(threadIdx.x >> 6);
  const int b = blockIdx.x, xcd = b & 7, r = b >> 3;
  const int kh = xcd & 1, mi = r >> 2, j = r & 3;
  const int panel = (xcd >> 1) * 4 + (mi >> 1), part = mi & 1;
  STAMP(0);
  if (w < 4) {
    if (j < 3) phaseA_compute<true>(L, w, lane, ws, panel, part, kh, j);
    else phaseA_compute<false>(L, w, lane, ws, panel, part, kh, j);
  } else {
    const u16* W = part ? sim : sre;
    if (j < 3) phaseA_load<true>(L, w - 4, lane, W, x, ws, panel, kh, j);
    else phaseA_load<false>(L, w - 4, lane, W, x, ws, panel, kh, j);
  }
}

DI bf16x8 ldW2(LAS unsigned char* p0, LAS unsigned char* p1) {
  s16x4 a = *(LAS s16x4*)p0, b = *(LAS s16x4*)p1;
  return __builtin_shufflevector(a, b, 0, 1, 2, 3, 4, 5, 6, 7);
}
__global__ __launch_bounds__(512, 2) void kB(const u16* __restrict__ dre, const u16* __restrict__ dim, void* ws, u16* __restrict__ y) {
  __shared__ __attribute__((aligned(16))) unsigned char smem[NSLOT * SLOT];
  LAS unsigned char* L = (LAS unsigned char*)smem;
  const int lane = threadIdx.x & 63, w = __builtin_amdgcn_readfirstlane(threadIdx.x >> 6);
  const int b = blockIdx.x, xcd = b & 7, r = b >> 3;
  const int mi = r >> 2, nb = r & 3;
  const int panel = (xcd >> 1) * 4 + (mi >> 1), mb = (xcd & 1) * 2 + (mi & 1);
  STAMP(8);
  if (w >= 4) {
    const int lw = w - 4;
    const u16* Wd = (lw >> 1) ? dim : dre;
    const __amdgpu_buffer_rsrc_t rd = __builtin_amdgcn_make_buffer_rsrc((void*)Wd, 0, DH * DH * 2, RSRC_W3);
    const int vd = (lane >> 3) * 128 + (((lane & 7) ^ ((4 * (lw & 1) + (lane >> 4)) & 7)) << 4);
    int sd = nb * 112 * 896 + (lw & 1) * 1024;
    auto issue = [&](int so) {
      LAS unsigned char* B = L + so + (lw >> 1) * 14336 + (lw & 1) * 1024;
#pragma unroll
      for (int q = 0; q < 7; ++q) dma16(rd, B + q * 2048, vd, sd + q * 2048);
      sd += 14336;
    };
    for (int s = 0; s < 5; ++s) issue(s * SLOT);
    WAITV(21);
    BAR();
    BAR();
    issue(0); issue(SLOT);
    WAITV(0);
    BAR();
    BAR();
    BAR();
  } else {
    const int kq = w, part = kq >> 1, k0 = 7 * (kq & 1);
    const __amdgpu_buffer_rsrc_t rs = __builtin_amdgcn_make_buffer_rsrc(ws, 0, PQ_BYTES, RSRC_W3);
    v4u pq[7][2][2];
#pragma unroll
    for (int ki = 0; ki < 7; ++ki)
#pragma unroll
      for (int kh = 0; kh < 2; ++kh)
#pragma unroll
        for (int ti = 0; ti < 2; ++ti)
          pq[ki][kh][ti] = __builtin_amdgcn_raw_buffer_load_b128(rs, lane * 16, ((((panel * 8 + mb * 2 + ti) * 2 + part) * 2 + kh) * 14 + k0 + ki) * 1024, 0);
    const int g = lane >> 4, u = g >> 1, h = g & 1, e = lane & 15;
    int wa[7];
#pragma unroll
    for (int ki = 0; ki < 7; ++ki) {
      const int kap = k0 + ki, Ln = 7 * e + (kap >> 1), c = 4 * (kap & 1) + 2 * u;
      wa[ki] = part * 14336 + Ln * 128 + ((c ^ ((Ln >> 1) & 7)) << 4) + 8 * h;
    }
    BAR();
    bf16x8 wr[2][7];
#pragma unroll
    for (int q = 0; q < 2; ++q)
#pragma unroll
      for (int ki = 0; ki < 7; ++ki) wr[q][ki] = ldW2(L + q * SLOT + wa[ki], L + q * SLOT + (wa[ki] ^ 16));
    WAITL0();
    BAR();
    f32x4 acc[7][2];
#pragma unroll
    for (int q = 0; q < 7; ++q) { acc[q][0] = f32x4{0, 0, 0, 0}; acc[q][1] = f32x4{0, 0, 0, 0}; }
#pragma unroll
    for (int ki = 0; ki < 7; ++ki)
#pragma unroll
      for (int q = 0; q < 2; ++q)
#pragma unroll
        for (int kh = 0; kh < 2; ++kh)
#pragma unroll
          for (int ti = 0; ti < 2; ++ti)
            acc[q][ti] = __builtin_amdgcn_mfma_f32_16x16x32_bf16(wr[q][ki], __builtin_bit_cast(bf16x8, pq[ki][kh][ti]), acc[q][ti], 0, 0, 0);
    BAR();
    STAMP(9);
#pragma unroll
    for (int q = 2; q < 7; ++q) {
      const int so = (q % 5) * SLOT;
#pragma unroll
      for (int ki = 0; ki < 7; ++ki) {
        bf16x8 wf = ldW2(L + so + wa[ki], L + so + (wa[ki] ^ 16));
#pragma unroll
        for (int kh = 0; kh < 2; ++kh)
#pragma unroll
          for (int ti = 0; ti < 2; ++ti)
            acc[q][ti] = __builtin_amdgcn_mfma_f32_16x16x32_bf16(wf, __builtin_bit_cast(bf16x8, pq[ki][kh][ti]), acc[q][ti], 0, 0, 0);
      }
    }
    WAITL0();
    STAMP(10);
    BAR();
#pragma unroll
    for (int q = 0; q < 7; ++q)
#pragma unroll
      for (int ti = 0; ti < 2; ++ti) *(LAS f32x4*)(L + kq * (32 * 464) + (ti * 16 + e) * 464 + (16 * q + 4 * g) * 4) = acc[q][ti];
    WAITL0();
    BAR();
  }
  const int i = threadIdx.x;
  if (i < 448) {
    const int row = i / 14, pc = i % 14;
    f32x4 s0 = {0, 0, 0, 0}, s1 = {0, 0, 0, 0};
#pragma unroll
    for (int kq = 0; kq < 4; ++kq) {
      s0 += *(LAS f32x4*)(L + kq * (32 * 464) + row * 464 + pc * 32);
      s1 += *(LAS f32x4*)(L + kq * (32 * 464) + row * 464 + pc * 32 + 16);
    }
    v4u o = {pk2(s0[0], s0[1]), pk2(s0[2], s0[3]), pk2(s1[0], s1[1]), pk2(s1[2], s1[3])};
    *(GAS v4u*)(y + (size_t)(panel * 128 + mb * 32 + row) * DH + nb * 112 + pc * 8) = o;
  }
  STAMP(11);
}

extern "C" void kernel_launch(void* const* d_in, const int* in_sizes, int n_in, void* d_out, int out_size, void* d_ws, size_t ws_size,
                              hipStream_t stream) {
  kA<<<256, 512, 0, stream>>>((const u16*)d_in[0], (const u16*)d_in[1], (const u16*)d_in[2], d_ws);
  kB<<<256, 512, 0, stream>>>((const u16*)d_in[3], (const u16*)d_in[4], d_ws, (u16*)d_out);
}
```

```cpp
#define AUXW 2
#include <hip/hip_runtime.h>
#include <hip/hip_bf16.h>
typedef unsigned short u16;
typedef short s16x4 __attribute__((ext_vector_type(4)));
typedef short bf16x8 __attribute__((ext_vector_type(8)));
typedef float f32x4 __attribute__((ext_vector_type(4)));
typedef float f32x16 __attribute__((ext_vector_type(16)));
typedef unsigned v4u __attribute__((ext_vector_type(4)));
typedef unsigned v2u __attribute__((ext_vector_type(2)));
typedef __bf16 bf2 __attribute__((ext_vector_type(2)));
typedef float f32x2 __attribute__((ext_vector_type(2)));
#define GAS __attribute__((address_space(1)))
#define LAS __attribute__((address_space(3)))
#define DI __device__ __forceinline__

constexpr int SEQ = 2048, DH = 448;
constexpr int SLOT = 32768, NSLOT = 5;
constexpr int PQ_BYTES = 16 * 8 * 2 * 2 * 14 * 1024;
constexpr int RSRC_W3 = 0x00020000;

#define STAMP(i)

DI unsigned pk2(float a, float b) { bf2 r = __builtin_convertvector(f32x2{a, b}, bf2); return __builtin_bit_cast(unsigned, r); }
template <int AUX = 0> DI void dma16(__amdgpu_buffer_rsrc_t rs, LAS unsigned char* dst, int voff, int soff) {
  __builtin_amdgcn_raw_ptr_buffer_load_lds(rs, (LAS void*)dst, 16, voff, soff, 0, AUX);
}
#define AUXX 0
DI s16x4 trd(LAS unsigned char* p) { return __builtin_amdgcn_ds_read_tr16_b64_v4i16((LAS s16x4*)p); }
#define BAR() __builtin_amdgcn_s_barrier()
#define WAITV(n) asm volatile("s_waitcnt vmcnt(" #n ")" ::: "memory")
#define WAITL0() asm volatile("s_waitcnt lgkmcnt(0)" ::: "memory")
#define SB() __builtin_amdgcn_sched_barrier(0)
#define SGB(m, n) __builtin_amdgcn_sched_group_barrier(m, n, 0)
#define ILV() do { SGB(0x8, 1); SGB(0x100, 2); SGB(0x8, 1); SGB(0x100, 2); SGB(0x8, 1); SGB(0x100, 2); SGB(0x8, 1); } while (0)

template <bool WIDE> struct Frags { bf16x8 w[2]; bf16x8 x[WIDE ? 2 : 1]; };

template <bool WIDE>
DI void loadFrags(Frags<WIDE>& f, LAS unsigned char* wA, LAS unsigned char* x0, LAS unsigned char* x1, int ks) {
  f.w[0] = *(LAS bf16x8*)(wA);
  f.w[1] = *(LAS bf16x8*)(wA + 4096);
  constexpr int RP = WIDE ? 256 : 128;
  {
    s16x4 a = trd(x0 + (16 * ks) * RP), b = trd(x0 + (16 * ks + 4) * RP);
    f.x[0] = __builtin_shufflevector(a, b, 0, 1, 2, 3, 4, 5, 6, 7);
  }
  if constexpr (WIDE) {
    s16x4 a = trd(x1 + (16 * ks) * RP), b = trd(x1 + (16 * ks + 4) * RP);
    f.x[1] = __builtin_shufflevector(a, b, 0, 1, 2, 3, 4, 5, 6, 7);
  }
}
template <bool WIDE> DI void mma(f32x16 (&acc)[WIDE ? 2 : 1][2], const Frags<WIDE>& f) {
#pragma unroll
  for (int dd = 0; dd < (WIDE ? 2 : 1); ++dd)
#pragma unroll
    for (int tt = 0; tt < 2; ++tt) acc[dd][tt] = __builtin_amdgcn_mfma_f32_32x32x16_bf16(f.x[dd], f.w[tt], acc[dd][tt], 0, 0, 0);
}

template <bool WIDE>
DI void phaseA_compute(LAS unsigned char* L, int cw, int lane, void* ws, int panel, int part, int kh, int j) {
  const int wt = cw & 1, wd = cw >> 1, h = lane >> 5, r32 = lane & 31;
  const int f = (lane >> 1) & 7;
  int wo[4];
#pragma unroll
  for (int ks = 0; ks < 4; ++ks) wo[ks] = (wt * 64 + r32) * 128 + (((2 * ks + h) ^ f) << 4);
  const int blk = (lane >> 4) & 1, q = (lane & 15) >> 2, p = lane & 3;
  int xo0, xo1;
  if constexpr (WIDE) {
    const int ch = wd * 8 + 2 * blk + (p >> 1);
    xo0 = 16384 + (8 * h + q) * 256 + ((ch ^ (q << 2)) << 4) + 8 * (p & 1);
    xo1 = xo0 ^ 64;
  } else {
    const int ch = wd * 4 + 2 * blk + (p >> 1);
    xo0 = 16384 + (8 * h + q) * 128 + ((ch ^ (((q >> 1) & 1) << 2)) << 4) + 8 * (p & 1);
    xo1 = xo0;
  }
  f32x16 acc[WIDE ? 2 : 1][2];
#pragma unroll
  for (int dd = 0; dd < (WIDE ? 2 : 1); ++dd)
#pragma unroll
    for (int tt = 0; tt < 2; ++tt)
#pragma unroll
      for (int i = 0; i < 16; ++i) acc[dd][tt][i] = 0.f;
  Frags<WIDE> F0, F1;
  int so = 0;
  BAR();
  STAMP(1);
  loadFrags<WIDE>(F0, L + wo[0], L + xo0, L + xo1, 0);
#pragma unroll 1
  for (int s = 0; s < 16; ++s) {
    LAS unsigned char* B = L + so;
    loadFrags<WIDE>(F1, B + wo[1], B + xo0, B + xo1, 1);
    mma<WIDE>(acc, F0); ILV(); SB();
    loadFrags<WIDE>(F0, B + wo[2], B + xo0, B + xo1, 2);
    mma<WIDE>(acc, F1); ILV(); SB();
    loadFrags<WIDE>(F1, B + wo[3], B + xo0, B + xo1, 3);
    mma<WIDE>(acc, F0); ILV(); SB();
    so = (so == (NSLOT - 1) * SLOT) ? 0 : so + SLOT;
    B = L + so;
    WAITL0(); BAR();
    SB();
    loadFrags<WIDE>(F0, B + wo[0], B + xo0, B + xo1, 0);
    mma<WIDE>(acc, F1); ILV(); SB();
  }
  STAMP(2);
  const __amdgpu_buffer_rsrc_t rs = __builtin_amdgcn_make_buffer_rsrc(ws, 0, PQ_BYTES, RSRC_W3);
  const float sg = part ? -1.f : 1.f;
#pragma unroll
  for (int dd = 0; dd < (WIDE ? 2 : 1); ++dd)
#pragma unroll
    for (int tt = 0; tt < 2; ++tt) {
      const int kap = WIDE ? 4 * j + 2 * wd + dd : 12 + wd;
      const int tau = wt * 4 + tt * 2 + (r32 >> 4);
      const int base = ((((panel * 8 + tau) * 2 + part) * 2 + kh) * 14 + kap) * 1024 + (h * 16 + (lane & 15)) * 16;
#pragma unroll
      for (int u = 0; u < 2; ++u) {
        v4u o;
#pragma unroll
        for (int i = 0; i < 4; ++i) o[i] = pk2(sg * acc[dd][tt][8 * u + 2 * i], sg * acc[dd][tt][8 * u + 2 * i + 1]);
        __builtin_amdgcn_raw_buffer_store_b128(o, rs, base + u * 512, 0, 0);
      }
    }
  STAMP(3);
}

template <bool WIDE>
DI void phaseA_load(LAS unsigned char* L, int lw, int lane, const u16* W, const u16* x, void* ws, int panel, int kh, int j) {
  const __amdgpu_buffer_rsrc_t rw = __builtin_amdgcn_make_buffer_rsrc((void*)W, 0, SEQ * SEQ * 2, RSRC_W3);
  const __amdgpu_buffer_rsrc_t rx = __builtin_amdgcn_make_buffer_rsrc((void*)x, 0, SEQ * DH * 2, RSRC_W3);
  const int vw = (lane >> 3) * 4096 + (((lane & 7) ^ ((4 * (lw & 1) + (lane >> 4)) & 7)) << 4);
  const int vx = WIDE ? (lane >> 4) * 896 + (((lane & 15) ^ ((lane >> 4) << 2)) << 4) : (lane >> 3) * 896 + (((lane & 7) ^ (((lane >> 4) & 1) << 2)) << 4);
  int sw = (panel * 128 + 8 * lw) * 4096 + kh * 2048;
  int sx = (kh * 1024 + (WIDE ? 4 : 8) * lw) * 896 + j * 256;
  auto issue = [&](int so) {
    LAS unsigned char* B = L + so + lw * 1024;
#pragma unroll
    for (int q = 0; q < 4; ++q) dma16<AUXW>(rw, B + q * 4096, vw, sw + q * 32 * 4096);
#pragma unroll
    for (int q = 0; q < (WIDE ? 4 : 2); ++q) dma16<AUXX>(rx, B + 16384 + q * 4096, vx, sx + q * (WIDE ? 16 : 32) * 896);
    sw += 128; sx += 64 * 896;
  };
#pragma unroll 1
  for (int s = 0; s < 4; ++s) issue(s * SLOT);
  STAMP(1);
  int so = 4 * SLOT;
#pragma unroll 1
  for (int s = 0; s < 12; ++s) {
    if constexpr (WIDE) WAITV(24); else WAITV(18);
    BAR();
    issue(so);
    so = (so == (NSLOT - 1) * SLOT) ? 0 : so + SLOT;
  }
  if constexpr (WIDE) WAITV(24); else WAITV(18);
  BAR();
  if constexpr (WIDE) WAITV(16); else WAITV(12);
  BAR();
  if constexpr (WIDE) WAITV(8); else WAITV(6);
  BAR();
  WAITV(0);
  STAMP(2);
  BAR();
  BAR();
}

__global__ __launch_bounds__(512, 2) void kA(const u16* __restrict__ x, const u16* __restrict__ sre, const u16* __restrict__ sim, void* ws) {
  __shared__ __attribute__((aligned(16))) unsigned char smem[NSLOT * SLOT];
  LAS unsigned char* L = (LAS unsigned char*)smem;
  const int lane = threadIdx.x & 63, w = __builtin_amdgcn_readfirstlane(threadIdx.x >> 6);
  const int b = blockIdx.x, xcd = b & 7, r = b >> 3;
  const int kh = xcd & 1, mi = r >> 2, j = r & 3;
  const int panel = (xcd >> 1) * 4 + (mi >> 1), part = mi & 1;
  STAMP(0);
  if (w < 4) {
    if (j < 3) phaseA_compute<true>(L, w, lane, ws, panel, part, kh, j);
    else phaseA_compute<false>(L, w, lane, ws, panel, part, kh, j);
  } else {
    const u16* W = part ? sim : sre;
    if (j < 3) phaseA_load<true>(L, w - 4, lane, W, x, ws, panel, kh, j);
    else phaseA_load<false>(L, w - 4, lane, W, x, ws, panel, kh, j);
  }
}

DI bf16x8 ldW2(LAS unsigned char* p0, LAS unsigned char* p1) {
  s16x4 a = *(LAS s16x4*)p0, b = *(LAS s16x4*)p1;
  return __builtin_shufflevector(a, b, 0, 1, 2, 3, 4, 5, 6, 7);
}
__global__ __launch_bounds__(512, 2) void kB(const u16* __restrict__ dre, const u16* __restrict__ dim, void* ws, u16* __restrict__ y) {
  __shared__ __attribute__((aligned(16))) unsigned char smem[NSLOT * SLOT];
  LAS unsigned char* L = (LAS unsigned char*)smem;
  const int lane = threadIdx.x & 63, w = __builtin_amdgcn_readfirstlane(threadIdx.x >> 6);
  const int b = blockIdx.x, xcd = b & 7, r = b >> 3;
  const int mi = r >> 2, nb = r & 3;
  const int panel = (xcd >> 1) * 4 + (mi >> 1), mb = (xcd & 1) * 2 + (mi & 1);
  STAMP(8);
  if (w >= 4) {
    const int lw = w - 4;
    const u16* Wd = (lw >> 1) ? dim : dre;
    const __amdgpu_buffer_rsrc_t rd = __builtin_amdgcn_make_buffer_rsrc((void*)Wd, 0, DH * DH * 2, RSRC_W3);
    const int vd = (lane >> 3) * 128 + (((lane & 7) ^ ((4 * (lw & 1) + (lane >> 4)) & 7)) << 4);
    int sd = nb * 112 * 896 + (lw & 1) * 1024;
    auto issue = [&](int so) {
      LAS unsigned char* B = L + so + (lw >> 1) * 14336 + (lw & 1) * 1024;
#pragma unroll
      for (int q = 0; q < 7; ++q) dma16(rd, B + q * 2048, vd, sd + q * 2048);
      sd += 14336;
    };
    for (int s = 0; s < 5; ++s) issue(s * SLOT);
    WAITV(21);
    BAR();
    BAR();
    issue(0); issue(SLOT);
    WAITV(0);
    BAR();
    BAR();
    BAR();
  } else {
    const int kq = w, part = kq >> 1, k0 = 7 * (kq & 1);
    const __amdgpu_buffer_rsrc_t rs = __builtin_amdgcn_make_buffer_rsrc(ws, 0, PQ_BYTES, RSRC_W3);
    v4u pq[7][2][2];
#pragma unroll
    for (int ki = 0; ki < 7; ++ki)
#pragma unroll
      for (int kh = 0; kh < 2; ++kh)
#pragma unroll
        for (int ti = 0; ti < 2; ++ti)
          pq[ki][kh][ti] = __builtin_amdgcn_raw_buffer_load_b128(rs, lane * 16, ((((panel * 8 + mb * 2 + ti) * 2 + part) * 2 + kh) * 14 + k0 + ki) * 1024, 0);
    const int g = lane >> 4, u = g >> 1, h = g & 1, e = lane & 15;
    int wa[7];
#pragma unroll
    for (int ki = 0; ki < 7; ++ki) {
      const int kap = k0 + ki, Ln = 7 * e + (kap >> 1), c = 4 * (kap & 1) + 2 * u;
      wa[ki] = part * 14336 + Ln * 128 + ((c ^ ((Ln >> 1) & 7)) << 4) + 8 * h;
    }
    BAR();
    bf16x8 wr[2][7];
#pragma unroll
    for (int q = 0; q < 2; ++q)
#pragma unroll
      for (int ki = 0; ki < 7; ++ki) wr[q][ki] = ldW2(L + q * SLOT + wa[ki], L + q * SLOT + (wa[ki] ^ 16));
    WAITL0();
    BAR();
    f32x4 acc[7][2];
#pragma unroll
    for (int q = 0; q < 7; ++q) { acc[q][0] = f32x4{0, 0, 0, 0}; acc[q][1] = f32x4{0, 0, 0, 0}; }
#pragma unroll
    for (int ki = 0; ki < 7; ++ki)
#pragma unroll
      for (int q = 0; q < 2; ++q)
#pragma unroll
        for (int kh = 0; kh < 2; ++kh)
#pragma unroll
          for (int ti = 0; ti < 2; ++ti)
            acc[q][ti] = __builtin_amdgcn_mfma_f32_16x16x32_bf16(wr[q][ki], __builtin_bit_cast(bf16x8, pq[ki][kh][ti]), acc[q][ti], 0, 0, 0);
    BAR();
    STAMP(9);
#pragma unroll
    for (int q = 2; q < 7; ++q) {
      const int so = (q % 5) * SLOT;
#pragma unroll
      for (int ki = 0; ki < 7; ++ki) {
        bf16x8 wf = ldW2(L + so + wa[ki], L + so + (wa[ki] ^ 16));
#pragma unroll
        for (int kh = 0; kh < 2; ++kh)
#pragma unroll
          for (int ti = 0; ti < 2; ++ti)
            acc[q][ti] = __builtin_amdgcn_mfma_f32_16x16x32_bf16(wf, __builtin_bit_cast(bf16x8, pq[ki][kh][ti]), acc[q][ti], 0, 0, 0);
      }
    }
    WAITL0();
    STAMP(10);
    BAR();
#pragma unroll
    for (int q = 0; q < 7; ++q)
#pragma unroll
      for (int ti = 0; ti < 2; ++ti) *(LAS f32x4*)(L + kq * (32 * 464) + (ti * 16 + e) * 464 + (16 * q + 4 * g) * 4) = acc[q][ti];
    WAITL0();
    BAR();
  }
  const int i = threadIdx.x;
  if (i < 448) {
    const int row = i / 14, pc = i % 14;
    f32x4 s0 = {0, 0, 0, 0}, s1 = {0, 0, 0, 0};
#pragma unroll
    for (int kq = 0; kq < 4; ++kq) {
      s0 += *(LAS f32x4*)(L + kq * (32 * 464) + row * 464 + pc * 32);
      s1 += *(LAS f32x4*)(L + kq * (32 * 464) + row * 464 + pc * 32 + 16);
    }
    v4u o = {pk2(s0[0], s0[1]), pk2(s0[2], s0[3]), pk2(s1[0], s1[1]), pk2(s1[2], s1[3])};
    *(GAS v4u*)(y + (size_t)(panel * 128 + mb * 32 + row) * DH + nb * 112 + pc * 8) = o;
  }
  STAMP(11);
}

extern "C" void kernel_launch(void* const* d_in, const int* in_sizes, int n_in, void* d_out, int out_size, void* d_ws, size_t ws_size,
                              hipStream_t stream) {
  kA<<<256, 512, 0, stream>>>((const u16*)d_in[0], (const u16*)d_in[1], (const u16*)d_in[2], d_ws);
  kB<<<256, 512, 0, stream>>>((const u16*)d_in[3], (const u16*)d_in[4], d_ws, (u16*)d_out);
}
```

```cpp
#define STAMPS
#define PROBE
#include <hip/hip_runtime.h>
#include <hip/hip_bf16.h>
typedef unsigned short u16;
typedef short s16x4 __attribute__((ext_vector_type(4)));
typedef short bf16x8 __attribute__((ext_vector_type(8)));
typedef float f32x4 __attribute__((ext_vector_type(4)));
typedef float f32x16 __attribute__((ext_vector_type(16)));
typedef unsigned v4u __attribute__((ext_vector_type(4)));
typedef unsigned v2u __attribute__((ext_vector_type(2)));
typedef __bf16 bf2 __attribute__((ext_vector_type(2)));
typedef float f32x2 __attribute__((ext_vector_type(2)));
#define GAS __attribute__((address_space(1)))
#define LAS __attribute__((address_space(3)))
#define DI __device__ __forceinline__

constexpr int SEQ = 2048, DH = 448;
constexpr int SLOT = 32768, NSLOT = 5;
constexpr int PQ_BYTES = 16 * 8 * 2 * 2 * 14 * 1024;
constexpr int RSRC_W3 = 0x00020000;

constexpr size_t STAMP_OFF = 64u << 20;
#define STAMP(i) do { if ((threadIdx.x & 63) == 0) { unsigned long long* sp_ = (unsigned long long*)((char*)ws + STAMP_OFF) + ((size_t)blockIdx.x * 8 + (threadIdx.x >> 6)) * 32; \
   sp_[2 * (i)] = __builtin_amdgcn_s_memrealtime(); sp_[2 * (i) + 1] = __builtin_amdgcn_s_memtime(); } } while (0)

DI unsigned pk2(float a, float b) { bf2 r = __builtin_convertvector(f32x2{a, b}, bf2); return __builtin_bit_cast(unsigned, r); }
template <int AUX = 0> DI void dma16(__amdgpu_buffer_rsrc_t rs, LAS unsigned char* dst, int voff, int soff) {
  __builtin_amdgcn_raw_ptr_buffer_load_lds(rs, (LAS void*)dst, 16, voff, soff, 0, AUX);
}
#define AUXW 0
#define AUXX 0
DI s16x4 trd(LAS unsigned char* p) { return __builtin_amdgcn_ds_read_tr16_b64_v4i16((LAS s16x4*)p); }
#define BAR() __builtin_amdgcn_s_barrier()
#define WAITV(n) asm volatile("s_waitcnt vmcnt(" #n ")" ::: "memory")
#define WAITL0() asm volatile("s_waitcnt lgkmcnt(0)" ::: "memory")
#define SB() __builtin_amdgcn_sched_barrier(0)
#define SGB(m, n) __builtin_amdgcn_sched_group_barrier(m, n, 0)
#define ILV() do { SGB(0x8, 1); SGB(0x100, 2); SGB(0x8, 1); SGB(0x100, 2); SGB(0x8, 1); SGB(0x100, 2); SGB(0x8, 1); } while (0)

template <bool WIDE> struct Frags { bf16x8 w[2]; bf16x8 x[WIDE ? 2 : 1]; };

template <bool WIDE>
DI void loadFrags(Frags<WIDE>& f, LAS unsigned char* wA, LAS unsigned char* x0, LAS unsigned char* x1, int ks) {
  f.w[0] = *(LAS bf16x8*)(wA);
  f.w[1] = *(LAS bf16x8*)(wA + 4096);
  constexpr int RP = WIDE ? 256 : 128;
  {
    s16x4 a = trd(x0 + (16 * ks) * RP), b = trd(x0 + (16 * ks + 4) * RP);
    f.x[0] = __builtin_shufflevector(a, b, 0, 1, 2, 3, 4, 5, 6, 7);
  }
  if constexpr (WIDE) {
    s16x4 a = trd(x1 + (16 * ks) * RP), b = trd(x1 + (16 * ks + 4) * RP);
    f.x[1] = __builtin_shufflevector(a, b, 0, 1, 2, 3, 4, 5, 6, 7);
  }
}
template <bool WIDE> DI void mma(f32x16 (&acc)[WIDE ? 2 : 1][2], const Frags<WIDE>& f) {
#pragma unroll
  for (int dd = 0; dd < (WIDE ? 2 : 1); ++dd)
#pragma unroll
    for (int tt = 0; tt < 2; ++tt) acc[dd][tt] = __builtin_amdgcn_mfma_f32_32x32x16_bf16(f.x[dd], f.w[tt], acc[dd][tt], 0, 0, 0);
}

template <bool WIDE>
DI void phaseA_compute(LAS unsigned char* L, int cw, int lane, void* ws, int panel, int part, int kh, int j) {
  const int wt = cw & 1, wd = cw >> 1, h = lane >> 5, r32 = lane & 31;
  const int f = (lane >> 1) & 7;
  int wo[4];
#pragma unroll
  for (int ks = 0; ks < 4; ++ks) wo[ks] = (wt * 64 + r32) * 128 + (((2 * ks + h) ^ f) << 4);
  const int blk = (lane >> 4) & 1, q = (lane & 15) >> 2, p = lane & 3;
  int xo0, xo1;
  if constexpr (WIDE) {
    const int ch = wd * 8 + 2 * blk + (p >> 1);
    xo0 = 16384 + (8 * h + q) * 256 + ((ch ^ (q << 2)) << 4) + 8 * (p & 1);
    xo1 = xo0 ^ 64;
  } else {
    const int ch = wd * 4 + 2 * blk + (p >> 1);
    xo0 = 16384 + (8 * h + q) * 128 + ((ch ^ (((q >> 1) & 1) << 2)) << 4) + 8 * (p & 1);
    xo1 = xo0;
  }
  f32x16 acc[WIDE ? 2 : 1][2];
#pragma unroll
  for (int dd = 0; dd < (WIDE ? 2 : 1); ++dd)
#pragma unroll
    for (int tt = 0; tt < 2; ++tt)
#pragma unroll
      for (int i = 0; i < 16; ++i) acc[dd][tt][i] = 0.f;
  Frags<WIDE> F0, F1;
  int so = 0;
  BAR();
  STAMP(1);
  loadFrags<WIDE>(F0, L + wo[0], L + xo0, L + xo1, 0);
#pragma unroll 1
  for (int s = 0; s < 16; ++s) {
    LAS unsigned char* B = L + so;
    loadFrags<WIDE>(F1, B + wo[1], B + xo0, B + xo1, 1);
    mma<WIDE>(acc, F0); ILV(); SB();
    loadFrags<WIDE>(F0, B + wo[2], B + xo0, B + xo1, 2);
    mma<WIDE>(acc, F1); ILV(); SB();
    loadFrags<WIDE>(F1, B + wo[3], B + xo0, B + xo1, 3);
    mma<WIDE>(acc, F0); ILV(); SB();
    so = (so == (NSLOT - 1) * SLOT) ? 0 : so + SLOT;
    B = L + so;
    WAITL0(); BAR();
    SB();
    loadFrags<WIDE>(F0, B + wo[0], B + xo0, B + xo1, 0);
    mma<WIDE>(acc, F1); ILV(); SB();
  }
  STAMP(2);
  const __amdgpu_buffer_rsrc_t rs = __builtin_amdgcn_make_buffer_rsrc(ws, 0, PQ_BYTES, RSRC_W3);
  const float sg = part ? -1.f : 1.f;
#pragma unroll
  for (int dd = 0; dd < (WIDE ? 2 : 1); ++dd)
#pragma unroll
    for (int tt = 0; tt < 2; ++tt) {
      const int kap = WIDE ? 4 * j + 2 * wd + dd : 12 + wd;
      const int tau = wt * 4 + tt * 2 + (r32 >> 4);
      const int base = ((((panel * 8 + tau) * 2 + part) * 2 + kh) * 14 + kap) * 1024 + (h * 16 + (lane & 15)) * 16;
#pragma unroll
      for (int u = 0; u < 2; ++u) {
        v4u o;
#pragma unroll
        for (int i = 0; i < 4; ++i) o[i] = pk2(sg * acc[dd][tt][8 * u + 2 * i], sg * acc[dd][tt][8 * u + 2 * i + 1]);
        __builtin_amdgcn_raw_buffer_store_b128(o, rs, base + u * 512, 0, 0);
      }
    }
  STAMP(3);
}

template <bool WIDE>
DI void phaseA_load(LAS unsigned char* L, int lw, int lane, const u16* W, const u16* x, void* ws, int panel, int kh, int j) {
  const __amdgpu_buffer_rsrc_t rw = __builtin_amdgcn_make_buffer_rsrc((void*)W, 0, SEQ * SEQ * 2, RSRC_W3);
  const __amdgpu_buffer_rsrc_t rx = __builtin_amdgcn_make_buffer_rsrc((void*)x, 0, SEQ * DH * 2, RSRC_W3);
  const int vw = (lane >> 3) * 4096 + (((lane & 7) ^ ((4 * (lw & 1) + (lane >> 4)) & 7)) << 4);
  const int vx = WIDE ? (lane >> 4) * 896 + (((lane & 15) ^ ((lane >> 4) << 2)) << 4) : (lane >> 3) * 896 + (((lane & 7) ^ (((lane >> 4) & 1) << 2)) << 4);
  int sw = (panel * 128 + 8 * lw) * 4096 + kh * 2048;
  int sx = (kh * 1024 + (WIDE ? 4 : 8) * lw) * 896 + j * 256;
  auto issue = [&](int so) {
    LAS unsigned char* B = L + so + lw * 1024;
#pragma unroll
    for (int q = 0; q < 4; ++q) dma16<AUXW>(rw, B + q * 4096, vw, sw + q * 32 * 4096);
#pragma unroll
    for (int q = 0; q < (WIDE ? 4 : 2); ++q) dma16<AUXX>(rx, B + 16384 + q * 4096, vx, sx + q * (WIDE ? 16 : 32) * 896);
    sw += 128; sx += 64 * 896;
  };
#pragma unroll 1
  for (int s = 0; s < 4; ++s) issue(s * SLOT);
  STAMP(1);
  int so = 4 * SLOT;
#pragma unroll 1
  for (int s = 0; s < 12; ++s) {
    if constexpr (WIDE) WAITV(24); else WAITV(18);
    BAR();
    issue(so);
    so = (so == (NSLOT - 1) * SLOT) ? 0 : so + SLOT;
  }
  if constexpr (WIDE) WAITV(24); else WAITV(18);
  BAR();
  if constexpr (WIDE) WAITV(16); else WAITV(12);
  BAR();
  if constexpr (WIDE) WAITV(8); else WAITV(6);
  BAR();
  WAITV(0);
  STAMP(2);
  BAR();
  BAR();
}

__global__ __launch_bounds__(512, 2) void kA(const u16* __restrict__ x, const u16* __restrict__ sre, const u16* __restrict__ sim, void* ws) {
  __shared__ __attribute__((aligned(16))) unsigned char smem[NSLOT * SLOT];
  LAS unsigned char* L = (LAS unsigned char*)smem;
  const int lane = threadIdx.x & 63, w = __builtin_amdgcn_readfirstlane(threadIdx.x >> 6);
  const int b = blockIdx.x, xcd = b & 7, r = b >> 3;
  const int kh = xcd & 1, mi = r >> 2, j = r & 3;
  const int panel = (xcd >> 1) * 4 + (mi >> 1), part = mi & 1;
  STAMP(0);
  if (w < 4) {
    if (j < 3) phaseA_compute<true>(L, w, lane, ws, panel, part, kh, j);
    else phaseA_compute<false>(L, w, lane, ws, panel, part, kh, j);
  } else {
    const u16* W = part ? sim : sre;
    if (j < 3) phaseA_load<true>(L, w - 4, lane, W, x, ws, panel, kh, j);
    else phaseA_load<false>(L, w - 4, lane, W, x, ws, panel, kh, j);
  }
}

DI bf16x8 ldW2(LAS unsigned char* p0, LAS unsigned char* p1) {
  s16x4 a = *(LAS s16x4*)p0, b = *(LAS s16x4*)p1;
  return __builtin_shufflevector(a, b, 0, 1, 2, 3, 4, 5, 6, 7);
}
__global__ __launch_bounds__(512, 2) void kB(const u16* __restrict__ dre, const u16* __restrict__ dim, void* ws, u16* __restrict__ y) {
  __shared__ __attribute__((aligned(16))) unsigned char smem[NSLOT * SLOT];
  LAS unsigned char* L = (LAS unsigned char*)smem;
  const int lane = threadIdx.x & 63, w = __builtin_amdgcn_readfirstlane(threadIdx.x >> 6);
  const int b = blockIdx.x, xcd = b & 7, r = b >> 3;
  const int mi = r >> 2, nb = r & 3;
  const int panel = (xcd >> 1) * 4 + (mi >> 1), mb = (xcd & 1) * 2 + (mi & 1);
  STAMP(8);
  if (w >= 4) {
    const int lw = w - 4;
    const u16* Wd = (lw >> 1) ? dim : dre;
    const __amdgpu_buffer_rsrc_t rd = __builtin_amdgcn_make_buffer_rsrc((void*)Wd, 0, DH * DH * 2, RSRC_W3);
    const int vd = (lane >> 3) * 128 + (((lane & 7) ^ ((4 * (lw & 1) + (lane >> 4)) & 7)) << 4);
    int sd = nb * 112 * 896 + (lw & 1) * 1024;
    auto issue = [&](int so) {
      LAS unsigned char* B = L + so + (lw >> 1) * 14336 + (lw & 1) * 1024;
#pragma unroll
      for (int q = 0; q < 7; ++q) dma16(rd, B + q * 2048, vd, sd + q * 2048);
      sd += 14336;
    };
    for (int s = 0; s < 5; ++s) issue(s * SLOT);
    WAITV(21);
    BAR();
    BAR();
    issue(0); issue(SLOT);
    WAITV(0);
    BAR();
    BAR();
    BAR();
  } else {
    const int kq = w, part = kq >> 1, k0 = 7 * (kq & 1);
    const __amdgpu_buffer_rsrc_t rs = __builtin_amdgcn_make_buffer_rsrc(ws, 0, PQ_BYTES, RSRC_W3);
    v4u pq[7][2][2];
#pragma unroll
    for (int ki = 0; ki < 7; ++ki)
#pragma unroll
      for (int kh = 0; kh < 2; ++kh)
#pragma unroll
        for (int ti = 0; ti < 2; ++ti)
          pq[ki][kh][ti] = __builtin_amdgcn_raw_buffer_load_b128(rs, lane * 16, ((((panel * 8 + mb * 2 + ti) * 2 + part) * 2 + kh) * 14 + k0 + ki) * 1024, 0);
    const int g = lane >> 4, u = g >> 1, h = g & 1, e = lane & 15;
    int wa[7];
#pragma unroll
    for (int ki = 0; ki < 7; ++ki) {
      const int kap = k0 + ki, Ln = 7 * e + (kap >> 1), c = 4 * (kap & 1) + 2 * u;
      wa[ki] = part * 14336 + Ln * 128 + ((c ^ ((Ln >> 1) & 7)) << 4) + 8 * h;
    }
    BAR();
    bf16x8 wr[2][7];
#pragma unroll
    for (int q = 0; q < 2; ++q)
#pragma unroll
      for (int ki = 0; ki < 7; ++ki) wr[q][ki] = ldW2(L + q * SLOT + wa[ki], L + q * SLOT + (wa[ki] ^ 16));
    WAITL0();
    BAR();
    f32x4 acc[7][2];
#pragma unroll
    for (int q = 0; q < 7; ++q) { acc[q][0] = f32x4{0, 0, 0, 0}; acc[q][1] = f32x4{0, 0, 0, 0}; }
#pragma unroll
    for (int ki = 0; ki < 7; ++ki)
#pragma unroll
      for (int q = 0; q < 2; ++q)
#pragma unroll
        for (int kh = 0; kh < 2; ++kh)
#pragma unroll
          for (int ti = 0; ti < 2; ++ti)
            acc[q][ti] = __builtin_amdgcn_mfma_f32_16x16x32_bf16(wr[q][ki], __builtin_bit_cast(bf16x8, pq[ki][kh][ti]), acc[q][ti], 0, 0, 0);
    BAR();
    STAMP(9);
#pragma unroll
    for (int q = 2; q < 7; ++q) {
      const int so = (q % 5) * SLOT;
#pragma unroll
      for (int ki = 0; ki < 7; ++ki) {
        bf16x8 wf = ldW2(L + so + wa[ki], L + so + (wa[ki] ^ 16));
#pragma unroll
        for (int kh = 0; kh < 2; ++kh)
#pragma unroll
          for (int ti = 0; ti < 2; ++ti)
            acc[q][ti] = __builtin_amdgcn_mfma_f32_16x16x32_bf16(wf, __builtin_bit_cast(bf16x8, pq[ki][kh][ti]), acc[q][ti], 0, 0, 0);
      }
    }
    WAITL0();
    STAMP(10);
    BAR();
#pragma unroll
    for (int q = 0; q < 7; ++q)
#pragma unroll
      for (int ti = 0; ti < 2; ++ti) *(LAS f32x4*)(L + kq * (32 * 464) + (ti * 16 + e) * 464 + (16 * q + 4 * g) * 4) = acc[q][ti];
    WAITL0();
    BAR();
  }
  const int i = threadIdx.x;
  if (i < 448) {
    const int row = i / 14, pc = i % 14;
    f32x4 s0 = {0, 0, 0, 0}, s1 = {0, 0, 0, 0};
#pragma unroll
    for (int kq = 0; kq < 4; ++kq) {
      s0 += *(LAS f32x4*)(L + kq * (32 * 464) + row * 464 + pc * 32);
      s1 += *(LAS f32x4*)(L + kq * (32 * 464) + row * 464 + pc * 32 + 16);
    }
    v4u o = {pk2(s0[0], s0[1]), pk2(s0[2], s0[3]), pk2(s1[0], s1[1]), pk2(s1[2], s1[3])};
    *(GAS v4u*)(y + (size_t)(panel * 128 + mb * 32 + row) * DH + nb * 112 + pc * 8) = o;
  }
  STAMP(11);
}

#define RT(b,w,i) (sp[((size_t)(b) * 8 + (w)) * 32 + 2 * (i)])
#define CY(b,w,i) (sp[((size_t)(b) * 8 + (w)) * 32 + 2 * (i) + 1])
__device__ unsigned long long minrt(const volatile unsigned long long* sp, int w, int i) { unsigned long long m = ~0ull; for (int b = 0; b < 256; ++b) { unsigned long long t = RT(b,w,i); m = t < m ? t : m; } return m; }
__device__ unsigned long long maxrt(const volatile unsigned long long* sp, int w, int i) { unsigned long long m = 0; for (int b = 0; b < 256; ++b) { unsigned long long t = RT(b,w,i); m = t > m ? t : m; } return m; }
__device__ unsigned long long avgd(const volatile unsigned long long* sp, int w, int i, int j) { unsigned long long m = 0; for (int b = 0; b < 256; ++b) { m += RT(b,w,j) - RT(b,w,i); } return m / 256; }
__device__ unsigned long long avgc(const volatile unsigned long long* sp, int w, int i, int j) { unsigned long long m = 0; for (int b = 0; b < 256; ++b) { m += CY(b,w,j) - CY(b,w,i); } return m / 256; }
#define MINRT(w,i) minrt(sp,w,i)
#define MAXRT(w,i) maxrt(sp,w,i)
#define AVGD(w,i,j) avgd(sp,w,i,j)
#define AVGC(w,i,j) avgc(sp,w,i,j)
__global__ void kProbe(void* ws) {
  const volatile unsigned long long* sp = (const volatile unsigned long long*)((char*)ws + STAMP_OFF);
  unsigned long long t0 = __builtin_amdgcn_s_memrealtime();
  unsigned long long val = (0ull);
  if (val > 5000) val = 5000;
  unsigned long long tend = t0 + 15000 + 10 * val;
  while (__builtin_amdgcn_s_memrealtime() < tend) __builtin_amdgcn_s_sleep(1);
}
extern "C" void kernel_launch(void* const* d_in, const int* in_sizes, int n_in, void* d_out, int out_size, void* d_ws, size_t ws_size,
                              hipStream_t stream) {
  kA<<<256, 512, 0, stream>>>((const u16*)d_in[0], (const u16*)d_in[1], (const u16*)d_in[2], d_ws);
  kB<<<256, 512, 0, stream>>>((const u16*)d_in[3], (const u16*)d_in[4], d_ws, (u16*)d_out);
  kProbe<<<1, 1, 0, stream>>>(d_ws);
}
```

```cpp
#include <hip/hip_runtime.h>
#include <hip/hip_bf16.h>
typedef unsigned short u16;
typedef unsigned long long u64;
typedef short s16x4 __attribute__((ext_vector_type(4)));
typedef short bf16x8 __attribute__((ext_vector_type(8)));
typedef float f32x4 __attribute__((ext_vector_type(4)));
typedef unsigned v4u __attribute__((ext_vector_type(4)));
typedef unsigned v2u __attribute__((ext_vector_type(2)));
typedef __bf16 bf2 __attribute__((ext_vector_type(2)));
typedef float f32x2 __attribute__((ext_vector_type(2)));
#define GAS __attribute__((address_space(1)))
#define LAS __attribute__((address_space(3)))
#define DI __device__ __forceinline__

constexpr int SEQ = 2048, DH = 448;
constexpr int SLOT = 32768;
constexpr int XSLOT = 4 * SLOT;
constexpr int MISC = XSLOT + 28672;
constexpr int LDS_BYTES = 163840;
constexpr int PQ_BYTES = 16 * 8 * 2 * 2 * 14 * 1024;
constexpr int FLAG_OFF = PQ_BYTES;
constexpr int EPOCH_OFF = FLAG_OFF + 256 * 256;
constexpr int WS_BYTES = EPOCH_OFF + 16 * 256;
constexpr int RSRC_W3 = 0x00020000;
constexpr int AUX_SC1 = 16;

#define STAMP(i)

DI unsigned pk2(float a, float b) { bf2 r = __builtin_convertvector(f32x2{a, b}, bf2); return __builtin_bit_cast(unsigned, r); }
template <int AUX = 0> DI void dma16(__amdgpu_buffer_rsrc_t rs, LAS unsigned char* dst, int voff, int soff) {
  __builtin_amdgcn_raw_ptr_buffer_load_lds(rs, (LAS void*)dst, 16, voff, soff, 0, AUX);
}
DI s16x4 trd(LAS unsigned char* p) { return __builtin_amdgcn_ds_read_tr16_b64_v4i16((LAS s16x4*)p); }
DI bf16x8 cat(s16x4 a, s16x4 b) { return __builtin_shufflevector(a, b, 0, 1, 2, 3, 4, 5, 6, 7); }
DI f32x4 mfma(bf16x8 a, bf16x8 b, f32x4 c) { return __builtin_amdgcn_mfma_f32_16x16x32_bf16(a, b, c, 0, 0, 0); }
#define TR(idx)
#define BAR() __builtin_amdgcn_s_barrier()
#define WAITV(n) asm volatile("s_waitcnt vmcnt(" #n ")" ::: "memory")
#define WAITL0() asm volatile("s_waitcnt lgkmcnt(0)" ::: "memory")
#define SB() __builtin_amdgcn_sched_barrier(0)
#define SGB(m, n) __builtin_amdgcn_sched_group_barrier(m, n, 0)

DI unsigned flagmin(v4u f) {
  unsigned a = f.x < f.y ? f.x : f.y, b = f.z < f.w ? f.z : f.w;
  return __builtin_amdgcn_readfirstlane(a < b ? a : b);
}
DI v4u ldflags(LAS unsigned char* p) { return *(volatile LAS v4u*)p; }
#define CB() asm volatile("" ::: "memory")
DI void waitflags(LAS unsigned char* p, unsigned need) {
  CB();
  while (flagmin(ldflags(p)) < need) __builtin_amdgcn_s_sleep(1);
  CB();
}
DI void stflag(LAS unsigned char* p, unsigned v, int lane) { CB(); if (lane == 0) *(volatile LAS unsigned*)p = v; CB(); }

#define KORDER(t, mi, j) ((((t) & 12) + (((t) + (j)) & 3) + 2 * (mi)) & 15)

template <int ND> struct Frags { bf16x8 w[4]; bf16x8 x[ND]; };

template <int ND>
DI void loadFrags(Frags<ND>& f, LAS unsigned char* wA, LAS unsigned char* (&xA)[ND], int so, int ks) {
  constexpr int RP = ND == 4 ? 256 : 128;
  f.x[0] = cat(trd(xA[0] + so + (32 * ks) * RP), trd(xA[0] + so + (32 * ks + 4) * RP));
#pragma unroll
  for (int tt = 0; tt < 4; ++tt) f.w[tt] = *(LAS bf16x8*)(wA + so + tt * 2048);
#pragma unroll
  for (int dd = 1; dd < ND; ++dd) f.x[dd] = cat(trd(xA[dd] + so + (32 * ks) * RP), trd(xA[dd] + so + (32 * ks + 4) * RP));
}
template <int ND> DI void mma(f32x4 (&acc)[ND][4], const Frags<ND>& f) {
#pragma unroll
  for (int dd = 0; dd < ND; ++dd)
#pragma unroll
    for (int tt = 0; tt < 4; ++tt) acc[dd][tt] = mfma(f.x[dd], f.w[tt], acc[dd][tt]);
}
template <int ND> DI void ilv() {
  if constexpr (ND == 4) {
#pragma unroll
    for (int i = 0; i < 13; ++i) { SGB(0x8, 1); SGB(0x100, 1); }
    SGB(0x8, 3);
  } else {
#pragma unroll
    for (int i = 0; i < 4; ++i) { SGB(0x8, 1); SGB(0x100, 2); }
    SGB(0x8, 1); SGB(0x100, 1); SGB(0x8, 3);
  }
}

template <int ND>
DI void phaseA_compute(LAS unsigned char* L, int cw, int lane, void* ws, __amdgpu_buffer_rsrc_t rs, int panel, int part, int kh, int j) {
  const int wt = cw & 1, wd = cw >> 1, g = lane >> 4, r16 = lane & 15;
  const int f = (lane >> 1) & 7;
  LAS unsigned char* wA0 = L + (wt * 64 + r16) * 128 + ((g ^ f) << 4);
  LAS unsigned char* wA1 = L + (wt * 64 + r16) * 128 + (((4 + g) ^ f) << 4);
  const int q = r16 >> 2, p = lane & 3;
  LAS unsigned char* xA[ND];
#pragma unroll
  for (int dd = 0; dd < ND; ++dd) {
    if constexpr (ND == 4) {
      const int u = wd * 4 + dd, hh = q | ((g & 1) << 2);
      xA[dd] = L + 16384 + (8 * g + q) * 256 + ((u ^ hh) << 5) + 8 * p;
    } else {
      const int u = wd * 2 + dd, hh = (q >> 1) | ((g & 1) << 1);
      xA[dd] = L + 16384 + (8 * g + q) * 128 + ((u ^ hh) << 5) + 8 * p;
    }
  }
  f32x4 acc[ND][4];
#pragma unroll
  for (int dd = 0; dd < ND; ++dd)
#pragma unroll
    for (int tt = 0; tt < 4; ++tt) acc[dd][tt] = f32x4{0.f, 0.f, 0.f, 0.f};
  Frags<ND> FA, FB;
  waitflags(L + MISC, 1);
  STAMP(1);
  loadFrags<ND>(FA, wA0, xA, 0, 0);
  SB();
#pragma unroll 1
  for (int s = 0; s < 16; ++s) {
    const int so = (s & 3) * SLOT;
    v4u fl = ldflags(L + MISC);
    loadFrags<ND>(FB, wA1, xA, so, 1);
    mma<ND>(acc, FA);
    ilv<ND>();
    SB();
    if (s < 15 && flagmin(fl) < (unsigned)(s + 2)) waitflags(L + MISC, s + 2);
    CB();
    SB();
    loadFrags<ND>(FA, wA0, xA, ((s + 1) & 3) * SLOT, 0);
    mma<ND>(acc, FB);
    ilv<ND>();
    SB();
    stflag(L + MISC + 16 + 4 * cw, s + 1, lane);
    SB();
  }
  STAMP(2);
  const float sg = part ? -1.f : 1.f;
#pragma unroll
  for (int c = 0; c < ND / 2; ++c)
#pragma unroll
    for (int tt = 0; tt < 4; ++tt) {
      const int kap = ND == 4 ? 4 * j + 2 * wd + c : 12 + wd;
      const int tau = wt * 4 + tt;
      const int base = ((((panel * 8 + tau) * 2 + part) * 2 + kh) * 14 + kap) * 1024;
      const f32x4 a = acc[2 * c][tt], b = acc[2 * c + 1][tt];
      v4u o = {pk2(sg * a[0], sg * a[1]), pk2(sg * a[2], sg * a[3]), pk2(sg * b[0], sg * b[1]), pk2(sg * b[2], sg * b[3])};
      __builtin_amdgcn_raw_buffer_store_b128(o, rs, lane * 16 + base, 0, AUX_SC1);
    }
}

template <int ND>
DI void loaderA(LAS unsigned char* L, int lw, int lane, const u16* W, const u16* x, void* ws, int panel, int kh, int j, int mi) {
  const __amdgpu_buffer_rsrc_t rw = __builtin_amdgcn_make_buffer_rsrc((void*)W, 0, SEQ * SEQ * 2, RSRC_W3);
  const __amdgpu_buffer_rsrc_t rx = __builtin_amdgcn_make_buffer_rsrc((void*)x, 0, SEQ * DH * 2, RSRC_W3);
  const int vw = (lane >> 3) * 4096 + (((lane & 7) ^ ((4 * (lw & 1) + (lane >> 4)) & 7)) << 4);
  int vx;
  if constexpr (ND == 4) vx = (lane >> 4) * 896 + (((lane & 15) ^ (((lane >> 4) | (((lw >> 1) & 1) << 2)) << 1)) << 4);
  else vx = (lane >> 3) * 896 + (((lane & 7) ^ ((((lane >> 4) & 1) | ((lw & 1) << 1)) << 1)) << 4);
  const int sw0 = (panel * 128 + 8 * lw) * 4096 + kh * 2048;
  const int sx0 = (kh * 1024 + (ND == 4 ? 4 : 8) * lw) * 896 + j * 256;
#pragma unroll 1
  for (int n = 0; n < 16; ++n) {
    if (lw == 0) TR(3 * n);
    if (n >= 4) waitflags(L + MISC + 16, n - 3);
    if (lw == 0) TR(3 * n + 1);
    const int kst = KORDER(n, mi, j);
    LAS unsigned char* B = L + (n & 3) * SLOT + lw * 1024;
    const int sw = sw0 + kst * 128, sx = sx0 + kst * 64 * 896;
#pragma unroll
    for (int q = 0; q < 4; ++q) dma16(rw, B + q * 4096, vw, sw + q * 32 * 4096);
#pragma unroll
    for (int q = 0; q < ND; ++q) dma16(rx, B + 16384 + q * 4096, vx, sx + q * (ND == 4 ? 16 : 32) * 896);
    if (lw == 0) TR(3 * n + 2);
    if (n == 0) { BAR(); }
    else {
      if constexpr (ND == 4) WAITV(8); else WAITV(6);
      stflag(L + MISC + 4 * lw, n, lane);
    }
  }
}

__global__ __launch_bounds__(512, 2) void fnet(const u16* __restrict__ x, const u16* __restrict__ sre, const u16* __restrict__ sim,
                                               const u16* __restrict__ dre, const u16* __restrict__ dim, void* ws, u16* __restrict__ y) {
  __shared__ __attribute__((aligned(16))) unsigned char smem[LDS_BYTES];
  LAS unsigned char* L = (LAS unsigned char*)smem;
  const int lane = threadIdx.x & 63, w = __builtin_amdgcn_readfirstlane(threadIdx.x >> 6);
  const int b = blockIdx.x, xcd = b & 7, r = b >> 3;
  const int kh = xcd & 1, mi = r >> 2, j = r & 3;
  const int panel = (xcd >> 1) * 4 + (mi >> 1), part = mi & 1;
  const int mb = kh * 2 + part, nb = j;
  const int erot = mi % 7;
  STAMP(0);
  if (w >= 4) {
    const int lw = w - 4;
    const u16* W = part ? sim : sre;
    if (j < 3) loaderA<4>(L, lw, lane, W, x, ws, panel, kh, j, mi);
    else loaderA<2>(L, lw, lane, W, x, ws, panel, kh, j, mi);
    STAMP(1);
    const u16* Wd = (lw >> 1) ? dim : dre;
    const __amdgpu_buffer_rsrc_t rd = __builtin_amdgcn_make_buffer_rsrc((void*)Wd, 0, DH * DH * 2, RSRC_W3);
    const int vd = (lane >> 3) * 128 + (((lane & 7) ^ ((4 * (lw & 1) + (lane >> 4)) & 7)) << 4);
    const int sd0 = nb * 112 * 896 + (lw & 1) * 1024;
    int et = erot;
#pragma unroll 1
    for (int q = 0; q < 7; ++q) {
      if (q >= 1) waitflags(L + MISC + 16, 12 + q);
      const int so = (q == 0 || q == 5) ? XSLOT : (q == 6 ? 0 : (q - 1) * SLOT);
      LAS unsigned char* B = L + so + (lw >> 1) * 14336 + (lw & 1) * 1024;
      const int sd = sd0 + et * 14336;
#pragma unroll
      for (int i = 0; i < 7; ++i) dma16(rd, B + i * 2048, vd, sd + i * 2048);
      et = et == 6 ? 0 : et + 1;
      WAITV(7);
      stflag(L + MISC + 4 * lw, 16 + q, lane);
    }
    WAITV(0);
    stflag(L + MISC + 4 * lw, 23, lane);
    STAMP(2);
    BAR();
    BAR();
  } else {
    const int cw = w;
    const __amdgpu_buffer_rsrc_t rs = __builtin_amdgcn_make_buffer_rsrc(ws, 0, WS_BYTES, RSRC_W3);
    const v2u ep = __builtin_amdgcn_raw_buffer_load_b64(rs, 0, EPOCH_OFF + panel * 256, AUX_SC1);
    if (threadIdx.x < 8) *(volatile LAS unsigned*)(L + MISC + 4 * threadIdx.x) = 0u;
    WAITL0();
    BAR();
    u64 tag = ((u64)__builtin_amdgcn_readfirstlane(ep.y) << 32 | __builtin_amdgcn_readfirstlane(ep.x)) + 1;
    if (j < 3) phaseA_compute<4>(L, cw, lane, ws, rs, panel, part, kh, j);
    else phaseA_compute<2>(L, cw, lane, ws, rs, panel, part, kh, j);
    STAMP(3);
    const int kq = cw, pb = kq >> 1, k0 = 7 * (kq & 1);
    const int g = lane >> 4, e = lane & 15;
    const int krot = 2 * nb;
    int wa[7];
#pragma unroll
    for (int ki = 0; ki < 7; ++ki) {
      int kk = ki + krot; kk = kk >= 7 ? kk - 7 : kk;
      const int kap = k0 + kk, Ln = 7 * e + (kap >> 1), c = 4 * (kap & 1) + (g >> 1);
      wa[ki] = pb * 14336 + Ln * 128 + ((c ^ ((Ln >> 1) & 7)) << 4) + 8 * (g & 1);
    }
    bf16x8 wr[2][7];
    waitflags(L + MISC, 17);
#pragma unroll
    for (int ki = 0; ki < 7; ++ki) wr[0][ki] = cat(*(LAS s16x4*)(L + XSLOT + wa[ki]), *(LAS s16x4*)(L + XSLOT + (wa[ki] ^ 32)));
    WAITL0();
    stflag(L + MISC + 16 + 4 * cw, 17, lane);
    waitflags(L + MISC, 18);
#pragma unroll
    for (int ki = 0; ki < 7; ++ki) wr[1][ki] = cat(*(LAS s16x4*)(L + wa[ki]), *(LAS s16x4*)(L + (wa[ki] ^ 32)));
    WAITL0();
    stflag(L + MISC + 16 + 4 * cw, 18, lane);
    STAMP(4);
    WAITV(0);
    STAMP(5);
    {
      const int wt = cw & 1, wd = cw >> 1;
      const int cmb = 2 * wt + ((lane >> 2) & 1), cnb = lane & 3;
      const int cons = (panel * 4 + cmb) * 4 + cnb;
      const int ent = (part * 2 + kh) * 8 + 2 * j + wd;
      if (lane < 8) __builtin_amdgcn_raw_buffer_store_b64(v2u{(unsigned)tag, (unsigned)(tag >> 32)}, rs, cons * 256 + ent * 8, FLAG_OFF, AUX_SC1);
    }
    {
      const int cons = (panel * 4 + mb) * 4 + nb;
      const int jw = lane & 7;
      const bool need = lane < 16 && ((kq & 1) ? jw >= 3 : jw <= 3);
      unsigned spins = 0;
      for (;;) {
        const v2u v = __builtin_amdgcn_raw_buffer_load_b64(rs, (lane & 15) * 8, FLAG_OFF + cons * 256 + pb * 128, AUX_SC1);
        const bool ok = !need || (v.x == (unsigned)tag && v.y == (unsigned)(tag >> 32));
        if (__all(ok) || ++spins > (1u << 16)) break;
        __builtin_amdgcn_s_sleep(1);
      }
      __builtin_amdgcn_fence(__ATOMIC_ACQUIRE, "wavefront");
    }
    STAMP(6);
    v4u pq[7][2][2];
#pragma unroll
    for (int ki = 0; ki < 7; ++ki) {
      int kk = ki + krot; kk = kk >= 7 ? kk - 7 : kk;
#pragma unroll
      for (int h2 = 0; h2 < 2; ++h2)
#pragma unroll
        for (int ti = 0; ti < 2; ++ti)
          pq[ki][h2][ti] = __builtin_amdgcn_raw_buffer_load_b128(rs, lane * 16, ((((panel * 8 + mb * 2 + ti) * 2 + pb) * 2 + h2) * 14 + k0 + kk) * 1024, AUX_SC1);
    }
    SB();
    waitflags(L + MISC, 23);
    f32x4 acc[7][2];
#pragma unroll
    for (int q = 0; q < 7; ++q) { acc[q][0] = f32x4{0, 0, 0, 0}; acc[q][1] = f32x4{0, 0, 0, 0}; }
#pragma unroll
    for (int ki = 0; ki < 7; ++ki) {
#pragma unroll
      for (int q = 0; q < 7; ++q) {
        bf16x8 wf;
        if (q < 2) wf = wr[q][ki];
        else {
          const int so = q == 5 ? XSLOT : (q == 6 ? 0 : (q - 1) * SLOT);
          wf = cat(*(LAS s16x4*)(L + so + wa[ki]), *(LAS s16x4*)(L + so + (wa[ki] ^ 32)));
        }
#pragma unroll
        for (int h2 = 0; h2 < 2; ++h2)
#pragma unroll
          for (int ti = 0; ti < 2; ++ti) acc[q][ti] = mfma(wf, __builtin_bit_cast(bf16x8, pq[ki][h2][ti]), acc[q][ti]);
      }
    }
    WAITL0();
    STAMP(7);
    BAR();
#pragma unroll
    for (int q = 0; q < 7; ++q) {
      int et = q + erot; et = et >= 7 ? et - 7 : et;
#pragma unroll
      for (int ti = 0; ti < 2; ++ti) *(LAS f32x4*)(L + kq * (32 * 464) + (ti * 16 + e) * 464 + (16 * et + 4 * g) * 4) = acc[q][ti];
    }
    WAITL0();
    BAR();
    if (threadIdx.x == 0 && mb == 0 && nb == 0)
      __builtin_amdgcn_raw_buffer_store_b64(v2u{(unsigned)tag, (unsigned)(tag >> 32)}, rs, 0, EPOCH_OFF + panel * 256, AUX_SC1);
  }
  const int i = threadIdx.x;
  if (i < 448) {
    const int row = i / 14, pc = i % 14;
    f32x4 s0 = {0, 0, 0, 0}, s1 = {0, 0, 0, 0};
#pragma unroll
    for (int kq = 0; kq < 4; ++kq) {
      s0 += *(LAS f32x4*)(L + kq * (32 * 464) + row * 464 + pc * 32);
      s1 += *(LAS f32x4*)(L + kq * (32 * 464) + row * 464 + pc * 32 + 16);
    }
    v4u o = {pk2(s0[0], s0[1]), pk2(s0[2], s0[3]), pk2(s1[0], s1[1]), pk2(s1[2], s1[3])};
    *(GAS v4u*)(y + (size_t)(panel * 128 + mb * 32 + row) * DH + nb * 112 + pc * 8) = o;
  }
  STAMP(8);
}

extern "C" void kernel_launch(void* const* d_in, const int* in_sizes, int n_in, void* d_out, int out_size, void* d_ws, size_t ws_size,
                              hipStream_t stream) {
  fnet<<<256, 512, 0, stream>>>((const u16*)d_in[0], (const u16*)d_in[1], (const u16*)d_in[2], (const u16*)d_in[3], (const u16*)d_in[4], d_ws, (u16*)d_out);
}
```

```cpp
#define STAMPS
#define PROBE
#include <hip/hip_runtime.h>
#include <hip/hip_bf16.h>
typedef unsigned short u16;
typedef unsigned long long u64;
typedef short s16x4 __attribute__((ext_vector_type(4)));
typedef short bf16x8 __attribute__((ext_vector_type(8)));
typedef float f32x4 __attribute__((ext_vector_type(4)));
typedef unsigned v4u __attribute__((ext_vector_type(4)));
typedef unsigned v2u __attribute__((ext_vector_type(2)));
typedef __bf16 bf2 __attribute__((ext_vector_type(2)));
typedef float f32x2 __attribute__((ext_vector_type(2)));
#define GAS __attribute__((address_space(1)))
#define LAS __attribute__((address_space(3)))
#define DI __device__ __forceinline__

constexpr int SEQ = 2048, DH = 448;
constexpr int SLOT = 32768;
constexpr int XSLOT = 4 * SLOT;
constexpr int MISC = XSLOT + 28672;
constexpr int LDS_BYTES = 163840;
constexpr int PQ_BYTES = 16 * 8 * 2 * 2 * 14 * 1024;
constexpr int FLAG_OFF = PQ_BYTES;
constexpr int EPOCH_OFF = FLAG_OFF + 256 * 256;
constexpr int WS_BYTES = EPOCH_OFF + 16 * 256;
constexpr int RSRC_W3 = 0x00020000;
constexpr int AUX_SC1 = 16;

constexpr size_t STAMP_OFF = 64u << 20;
#define STAMP(i) do { if ((threadIdx.x & 63) == 0) { unsigned long long* sp_ = (unsigned long long*)((char*)ws + STAMP_OFF) + ((size_t)blockIdx.x * 8 + (threadIdx.x >> 6)) * 32; \
   sp_[2 * (i)] = __builtin_amdgcn_s_memrealtime(); sp_[2 * (i) + 1] = __builtin_amdgcn_s_memtime(); } } while (0)

DI unsigned pk2(float a, float b) { bf2 r = __builtin_convertvector(f32x2{a, b}, bf2); return __builtin_bit_cast(unsigned, r); }
template <int AUX = 0> DI void dma16(__amdgpu_buffer_rsrc_t rs, LAS unsigned char* dst, int voff, int soff) {
  __builtin_amdgcn_raw_ptr_buffer_load_lds(rs, (LAS void*)dst, 16, voff, soff, 0, AUX);
}
DI s16x4 trd(LAS unsigned char* p) { return __builtin_amdgcn_ds_read_tr16_b64_v4i16((LAS s16x4*)p); }
DI bf16x8 cat(s16x4 a, s16x4 b) { return __builtin_shufflevector(a, b, 0, 1, 2, 3, 4, 5, 6, 7); }
DI f32x4 mfma(bf16x8 a, bf16x8 b, f32x4 c) { return __builtin_amdgcn_mfma_f32_16x16x32_bf16(a, b, c, 0, 0, 0); }
#define TR(idx)
#define BAR() __builtin_amdgcn_s_barrier()
#define WAITV(n) asm volatile("s_waitcnt vmcnt(" #n ")" ::: "memory")
#define WAITL0() asm volatile("s_waitcnt lgkmcnt(0)" ::: "memory")
#define SB() __builtin_amdgcn_sched_barrier(0)
#define SGB(m, n) __builtin_amdgcn_sched_group_barrier(m, n, 0)

DI unsigned flagmin(v4u f) {
  unsigned a = f.x < f.y ? f.x : f.y, b = f.z < f.w ? f.z : f.w;
  return __builtin_amdgcn_readfirstlane(a < b ? a : b);
}
DI v4u ldflags(LAS unsigned char* p) { return *(volatile LAS v4u*)p; }
#define CB() asm volatile("" ::: "memory")
DI void waitflags(LAS unsigned char* p, unsigned need) {
  CB();
  while (flagmin(ldflags(p)) < need) __builtin_amdgcn_s_sleep(1);
  CB();
}
DI void stflag(LAS unsigned char* p, unsigned v, int lane) { CB(); if (lane == 0) *(volatile LAS unsigned*)p = v; CB(); }

#define KORDER(t, mi, j) ((((t) & 12) + (((t) + (j)) & 3) + 2 * (mi)) & 15)

template <int ND> struct Frags { bf16x8 w[4]; bf16x8 x[ND]; };

template <int ND>
DI void loadFrags(Frags<ND>& f, LAS unsigned char* wA, LAS unsigned char* (&xA)[ND], int so, int ks) {
  constexpr int RP = ND == 4 ? 256 : 128;
  f.x[0] = cat(trd(xA[0] + so + (32 * ks) * RP), trd(xA[0] + so + (32 * ks + 4) * RP));
#pragma unroll
  for (int tt = 0; tt < 4; ++tt) f.w[tt] = *(LAS bf16x8*)(wA + so + tt * 2048);
#pragma unroll
  for (int dd = 1; dd < ND; ++dd) f.x[dd] = cat(trd(xA[dd] + so + (32 * ks) * RP), trd(xA[dd] + so + (32 * ks + 4) * RP));
}
template <int ND> DI void mma(f32x4 (&acc)[ND][4], const Frags<ND>& f) {
#pragma unroll
  for (int dd = 0; dd < ND; ++dd)
#pragma unroll
    for (int tt = 0; tt < 4; ++tt) acc[dd][tt] = mfma(f.x[dd], f.w[tt], acc[dd][tt]);
}
template <int ND> DI void ilv() {
  if constexpr (ND == 4) {
#pragma unroll
    for (int i = 0; i < 13; ++i) { SGB(0x8, 1); SGB(0x100, 1); }
    SGB(0x8, 3);
  } else {
#pragma unroll
    for (int i = 0; i < 4; ++i) { SGB(0x8, 1); SGB(0x100, 2); }
    SGB(0x8, 1); SGB(0x100, 1); SGB(0x8, 3);
  }
}

template <int ND>
DI void phaseA_compute(LAS unsigned char* L, int cw, int lane, void* ws, __amdgpu_buffer_rsrc_t rs, int panel, int part, int kh, int j) {
  const int wt = cw & 1, wd = cw >> 1, g = lane >> 4, r16 = lane & 15;
  const int f = (lane >> 1) & 7;
  LAS unsigned char* wA0 = L + (wt * 64 + r16) * 128 + ((g ^ f) << 4);
  LAS unsigned char* wA1 = L + (wt * 64 + r16) * 128 + (((4 + g) ^ f) << 4);
  const int q = r16 >> 2, p = lane & 3;
  LAS unsigned char* xA[ND];
#pragma unroll
  for (int dd = 0; dd < ND; ++dd) {
    if constexpr (ND == 4) {
      const int u = wd * 4 + dd, hh = q | ((g & 1) << 2);
      xA[dd] = L + 16384 + (8 * g + q) * 256 + ((u ^ hh) << 5) + 8 * p;
    } else {
      const int u = wd * 2 + dd, hh = (q >> 1) | ((g & 1) << 1);
      xA[dd] = L + 16384 + (8 * g + q) * 128 + ((u ^ hh) << 5) + 8 * p;
    }
  }
  f32x4 acc[ND][4];
#pragma unroll
  for (int dd = 0; dd < ND; ++dd)
#pragma unroll
    for (int tt = 0; tt < 4; ++tt) acc[dd][tt] = f32x4{0.f, 0.f, 0.f, 0.f};
  Frags<ND> FA, FB;
  waitflags(L + MISC, 1);
  STAMP(1);
  loadFrags<ND>(FA, wA0, xA, 0, 0);
  SB();
#pragma unroll 1
  for (int s = 0; s < 16; ++s) {
    const int so = (s & 3) * SLOT;
    v4u fl = ldflags(L + MISC);
    loadFrags<ND>(FB, wA1, xA, so, 1);
    mma<ND>(acc, FA);
    ilv<ND>();
    SB();
    if (s < 15 && flagmin(fl) < (unsigned)(s + 2)) waitflags(L + MISC, s + 2);
    CB();
    SB();
    loadFrags<ND>(FA, wA0, xA, ((s + 1) & 3) * SLOT, 0);
    mma<ND>(acc, FB);
    ilv<ND>();
    SB();
    stflag(L + MISC + 16 + 4 * cw, s + 1, lane);
    SB();
  }
  STAMP(2);
  const float sg = part ? -1.f : 1.f;
#pragma unroll
  for (int c = 0; c < ND / 2; ++c)
#pragma unroll
    for (int tt = 0; tt < 4; ++tt) {
      const int kap = ND == 4 ? 4 * j + 2 * wd + c : 12 + wd;
      const int tau = wt * 4 + tt;
      const int base = ((((panel * 8 + tau) * 2 + part) * 2 + kh) * 14 + kap) * 1024;
      const f32x4 a = acc[2 * c][tt], b = acc[2 * c + 1][tt];
      v4u o = {pk2(sg * a[0], sg * a[1]), pk2(sg * a[2], sg * a[3]), pk2(sg * b[0], sg * b[1]), pk2(sg * b[2], sg * b[3])};
      __builtin_amdgcn_raw_buffer_store_b128(o, rs, lane * 16 + base, 0, AUX_SC1);
    }
}

template <int ND>
DI void loaderA(LAS unsigned char* L, int lw, int lane, const u16* W, const u16* x, void* ws, int panel, int kh, int j, int mi) {
  const __amdgpu_buffer_rsrc_t rw = __builtin_amdgcn_make_buffer_rsrc((void*)W, 0, SEQ * SEQ * 2, RSRC_W3);
  const __amdgpu_buffer_rsrc_t rx = __builtin_amdgcn_make_buffer_rsrc((void*)x, 0, SEQ * DH * 2, RSRC_W3);
  const int vw = (lane >> 3) * 4096 + (((lane & 7) ^ ((4 * (lw & 1) + (lane >> 4)) & 7)) << 4);
  int vx;
  if constexpr (ND == 4) vx = (lane >> 4) * 896 + (((lane & 15) ^ (((lane >> 4) | (((lw >> 1) & 1) << 2)) << 1)) << 4);
  else vx = (lane >> 3) * 896 + (((lane & 7) ^ ((((lane >> 4) & 1) | ((lw & 1) << 1)) << 1)) << 4);
  const int sw0 = (panel * 128 + 8 * lw) * 4096 + kh * 2048;
  const int sx0 = (kh * 1024 + (ND == 4 ? 4 : 8) * lw) * 896 + j * 256;
#pragma unroll 1
  for (int n = 0; n < 16; ++n) {
    if (lw == 0) TR(3 * n);
    if (n >= 4) waitflags(L + MISC + 16, n - 3);
    if (lw == 0) TR(3 * n + 1);
    const int kst = KORDER(n, mi, j);
    LAS unsigned char* B = L + (n & 3) * SLOT + lw * 1024;
    const int sw = sw0 + kst * 128, sx = sx0 + kst * 64 * 896;
#pragma unroll
    for (int q = 0; q < 4; ++q) dma16(rw, B + q * 4096, vw, sw + q * 32 * 4096);
#pragma unroll
    for (int q = 0; q < ND; ++q) dma16(rx, B + 16384 + q * 4096, vx, sx + q * (ND == 4 ? 16 : 32) * 896);
    if (lw == 0) TR(3 * n + 2);
    if (n == 0) { BAR(); }
    else {
      if constexpr (ND == 4) WAITV(8); else WAITV(6);
      stflag(L + MISC + 4 * lw, n, lane);
    }
  }
}

__global__ __launch_bounds__(512, 2) void fnet(const u16* __restrict__ x, const u16* __restrict__ sre, const u16* __restrict__ sim,
                                               const u16* __restrict__ dre, const u16* __restrict__ dim, void* ws, u16* __restrict__ y) {
  __shared__ __attribute__((aligned(16))) unsigned char smem[LDS_BYTES];
  LAS unsigned char* L = (LAS unsigned char*)smem;
  const int lane = threadIdx.x & 63, w = __builtin_amdgcn_readfirstlane(threadIdx.x >> 6);
  const int b = blockIdx.x, xcd = b & 7, r = b >> 3;
  const int kh = xcd & 1, mi = r >> 2, j = r & 3;
  const int panel = (xcd >> 1) * 4 + (mi >> 1), part = mi & 1;
  const int mb = kh * 2 + part, nb = j;
  const int erot = mi % 7;
  STAMP(0);
  if (w >= 4) {
    const int lw = w - 4;
    const u16* W = part ? sim : sre;
    if (j < 3) loaderA<4>(L, lw, lane, W, x, ws, panel, kh, j, mi);
    else loaderA<2>(L, lw, lane, W, x, ws, panel, kh, j, mi);
    STAMP(1);
    const u16* Wd = (lw >> 1) ? dim : dre;
    const __amdgpu_buffer_rsrc_t rd = __builtin_amdgcn_make_buffer_rsrc((void*)Wd, 0, DH * DH * 2, RSRC_W3);
    const int vd = (lane >> 3) * 128 + (((lane & 7) ^ ((4 * (lw & 1) + (lane >> 4)) & 7)) << 4);
    const int sd0 = nb * 112 * 896 + (lw & 1) * 1024;
    int et = erot;
#pragma unroll 1
    for (int q = 0; q < 7; ++q) {
      if (q >= 1) waitflags(L + MISC + 16, 12 + q);
      const int so = (q == 0 || q == 5) ? XSLOT : (q == 6 ? 0 : (q - 1) * SLOT);
      LAS unsigned char* B = L + so + (lw >> 1) * 14336 + (lw & 1) * 1024;
      const int sd = sd0 + et * 14336;
#pragma unroll
      for (int i = 0; i < 7; ++i) dma16(rd, B + i * 2048, vd, sd + i * 2048);
      et = et == 6 ? 0 : et + 1;
      WAITV(7);
      stflag(L + MISC + 4 * lw, 16 + q, lane);
    }
    WAITV(0);
    stflag(L + MISC + 4 * lw, 23, lane);
    STAMP(2);
    BAR();
    BAR();
  } else {
    const int cw = w;
    const __amdgpu_buffer_rsrc_t rs = __builtin_amdgcn_make_buffer_rsrc(ws, 0, WS_BYTES, RSRC_W3);
    const v2u ep = __builtin_amdgcn_raw_buffer_load_b64(rs, 0, EPOCH_OFF + panel * 256, AUX_SC1);
    if (threadIdx.x < 8) *(volatile LAS unsigned*)(L + MISC + 4 * threadIdx.x) = 0u;
    WAITL0();
    BAR();
    u64 tag = ((u64)__builtin_amdgcn_readfirstlane(ep.y) << 32 | __builtin_amdgcn_readfirstlane(ep.x)) + 1;
    if (j < 3) phaseA_compute<4>(L, cw, lane, ws, rs, panel, part, kh, j);
    else phaseA_compute<2>(L, cw, lane, ws, rs, panel, part, kh, j);
    STAMP(3);
    const int kq = cw, pb = kq >> 1, k0 = 7 * (kq & 1);
    const int g = lane >> 4, e = lane & 15;
    const int krot = 2 * nb;
    int wa[7];
#pragma unroll
    for (int ki = 0; ki < 7; ++ki) {
      int kk = ki + krot; kk = kk >= 7 ? kk - 7 : kk;
      const int kap = k0 + kk, Ln = 7 * e + (kap >> 1), c = 4 * (kap & 1) + (g >> 1);
      wa[ki] = pb * 14336 + Ln * 128 + ((c ^ ((Ln >> 1) & 7)) << 4) + 8 * (g & 1);
    }
    bf16x8 wr[2][7];
    waitflags(L + MISC, 17);
#pragma unroll
    for (int ki = 0; ki < 7; ++ki) wr[0][ki] = cat(*(LAS s16x4*)(L + XSLOT + wa[ki]), *(LAS s16x4*)(L + XSLOT + (wa[ki] ^ 32)));
    WAITL0();
    stflag(L + MISC + 16 + 4 * cw, 17, lane);
    waitflags(L + MISC, 18);
#pragma unroll
    for (int ki = 0; ki < 7; ++ki) wr[1][ki] = cat(*(LAS s16x4*)(L + wa[ki]), *(LAS s16x4*)(L + (wa[ki] ^ 32)));
    WAITL0();
    stflag(L + MISC + 16 + 4 * cw, 18, lane);
    STAMP(4);
    WAITV(0);
    STAMP(5);
    {
      const int wt = cw & 1, wd = cw >> 1;
      const int cmb = 2 * wt + ((lane >> 2) & 1), cnb = lane & 3;
      const int cons = (panel * 4 + cmb) * 4 + cnb;
      const int ent = (part * 2 + kh) * 8 + 2 * j + wd;
      if (lane < 8) __builtin_amdgcn_raw_buffer_store_b64(v2u{(unsigned)tag, (unsigned)(tag >> 32)}, rs, cons * 256 + ent * 8, FLAG_OFF, AUX_SC1);
    }
    {
      const int cons = (panel * 4 + mb) * 4 + nb;
      const int jw = lane & 7;
      const bool need = lane < 16 && ((kq & 1) ? jw >= 3 : jw <= 3);
      unsigned spins = 0;
      for (;;) {
        const v2u v = __builtin_amdgcn_raw_buffer_load_b64(rs, (lane & 15) * 8, FLAG_OFF + cons * 256 + pb * 128, AUX_SC1);
        const bool ok = !need || (v.x == (unsigned)tag && v.y == (unsigned)(tag >> 32));
        if (__all(ok) || ++spins > (1u << 16)) break;
        __builtin_amdgcn_s_sleep(1);
      }
      __builtin_amdgcn_fence(__ATOMIC_ACQUIRE, "wavefront");
    }
    STAMP(6);
    v4u pq[7][2][2];
#pragma unroll
    for (int ki = 0; ki < 7; ++ki) {
      int kk = ki + krot; kk = kk >= 7 ? kk - 7 : kk;
#pragma unroll
      for (int h2 = 0; h2 < 2; ++h2)
#pragma unroll
        for (int ti = 0; ti < 2; ++ti)
          pq[ki][h2][ti] = __builtin_amdgcn_raw_buffer_load_b128(rs, lane * 16, ((((panel * 8 + mb * 2 + ti) * 2 + pb) * 2 + h2) * 14 + k0 + kk) * 1024, AUX_SC1);
    }
    SB();
    waitflags(L + MISC, 23);
    f32x4 acc[7][2];
#pragma unroll
    for (int q = 0; q < 7; ++q) { acc[q][0] = f32x4{0, 0, 0, 0}; acc[q][1] = f32x4{0, 0, 0, 0}; }
#pragma unroll
    for (int ki = 0; ki < 7; ++ki) {
#pragma unroll
      for (int q = 0; q < 7; ++q) {
        bf16x8 wf;
        if (q < 2) wf = wr[q][ki];
        else {
          const int so = q == 5 ? XSLOT : (q == 6 ? 0 : (q - 1) * SLOT);
          wf = cat(*(LAS s16x4*)(L + so + wa[ki]), *(LAS s16x4*)(L + so + (wa[ki] ^ 32)));
        }
#pragma unroll
        for (int h2 = 0; h2 < 2; ++h2)
#pragma unroll
          for (int ti = 0; ti < 2; ++ti) acc[q][ti] = mfma(wf, __builtin_bit_cast(bf16x8, pq[ki][h2][ti]), acc[q][ti]);
      }
    }
    WAITL0();
    STAMP(7);
    BAR();
#pragma unroll
    for (int q = 0; q < 7; ++q) {
      int et = q + erot; et = et >= 7 ? et - 7 : et;
#pragma unroll
      for (int ti = 0; ti < 2; ++ti) *(LAS f32x4*)(L + kq * (32 * 464) + (ti * 16 + e) * 464 + (16 * et + 4 * g) * 4) = acc[q][ti];
    }
    WAITL0();
    BAR();
    if (threadIdx.x == 0 && mb == 0 && nb == 0)
      __builtin_amdgcn_raw_buffer_store_b64(v2u{(unsigned)tag, (unsigned)(tag >> 32)}, rs, 0, EPOCH_OFF + panel * 256, AUX_SC1);
  }
  const int i = threadIdx.x;
  if (i < 448) {
    const int row = i / 14, pc = i % 14;
    f32x4 s0 = {0, 0, 0, 0}, s1 = {0, 0, 0, 0};
#pragma unroll
    for (int kq = 0; kq < 4; ++kq) {
      s0 += *(LAS f32x4*)(L + kq * (32 * 464) + row * 464 + pc * 32);
      s1 += *(LAS f32x4*)(L + kq * (32 * 464) + row * 464 + pc * 32 + 16);
    }
    v4u o = {pk2(s0[0], s0[1]), pk2(s0[2], s0[3]), pk2(s1[0], s1[1]), pk2(s1[2], s1[3])};
    *(GAS v4u*)(y + (size_t)(panel * 128 + mb * 32 + row) * DH + nb * 112 + pc * 8) = o;
  }
  STAMP(8);
}

#define RT(b,w,i) (sp[((size_t)(b) * 8 + (w)) * 32 + 2 * (i)])
#define CY(b,w,i) (sp[((size_t)(b) * 8 + (w)) * 32 + 2 * (i) + 1])
__device__ unsigned long long minrt(const volatile unsigned long long* sp, int w, int i) { unsigned long long m = ~0ull; for (int b = 0; b < 256; ++b) { unsigned long long t = RT(b,w,i); m = t < m ? t : m; } return m; }
__device__ unsigned long long maxrt(const volatile unsigned long long* sp, int w, int i) { unsigned long long m = 0; for (int b = 0; b < 256; ++b) { unsigned long long t = RT(b,w,i); m = t > m ? t : m; } return m; }
__device__ unsigned long long avgd(const volatile unsigned long long* sp, int w, int i, int j) { unsigned long long m = 0; for (int b = 0; b < 256; ++b) { m += RT(b,w,j) - RT(b,w,i); } return m / 256; }
__device__ unsigned long long avgc(const volatile unsigned long long* sp, int w, int i, int j) { unsigned long long m = 0; for (int b = 0; b < 256; ++b) { m += CY(b,w,j) - CY(b,w,i); } return m / 256; }
#define MINRT(w,i) minrt(sp,w,i)
#define MAXRT(w,i) maxrt(sp,w,i)
#define AVGD(w,i,j) avgd(sp,w,i,j)
#define AVGC(w,i,j) avgc(sp,w,i,j)
__global__ void kProbe(void* ws) {
  const volatile unsigned long long* sp = (const volatile unsigned long long*)((char*)ws + STAMP_OFF);
  unsigned long long t0 = __builtin_amdgcn_s_memrealtime();
  unsigned long long val = (0ull);
  if (val > 5000) val = 5000;
  unsigned long long tend = t0 + 15000 + 10 * val;
  while (__builtin_amdgcn_s_memrealtime() < tend) __builtin_amdgcn_s_sleep(1);
}
extern "C" void kernel_launch(void* const* d_in, const int* in_sizes, int n_in, void* d_out, int out_size, void* d_ws, size_t ws_size,
                              hipStream_t stream) {
  fnet<<<256, 512, 0, stream>>>((const u16*)d_in[0], (const u16*)d_in[1], (const u16*)d_in[2], (const u16*)d_in[3], (const u16*)d_in[4], d_ws, (u16*)d_out);
  kProbe<<<1, 1, 0, stream>>>(d_ws);
}
```

```cpp
#include <hip/hip_runtime.h>
#include <hip/hip_bf16.h>
typedef unsigned short u16;
typedef unsigned long long u64;
typedef short s16x4 __attribute__((ext_vector_type(4)));
typedef short bf16x8 __attribute__((ext_vector_type(8)));
typedef float f32x4 __attribute__((ext_vector_type(4)));
typedef float f32x16 __attribute__((ext_vector_type(16)));
typedef unsigned v4u __attribute__((ext_vector_type(4)));
typedef unsigned v2u __attribute__((ext_vector_type(2)));
typedef __bf16 bf2 __attribute__((ext_vector_type(2)));
typedef float f32x2 __attribute__((ext_vector_type(2)));
#define GAS __attribute__((address_space(1)))
#define LAS __attribute__((address_space(3)))
#define DI __device__ __forceinline__

#define AUXW 0
#define SCHED 2
#define SHAPE16 0
#define E0 4

constexpr int SEQ = 2048, DH = 448;
constexpr int SLOT = 32768;
constexpr int XSLOT = 4 * SLOT;
constexpr int LDS_BYTES = 163840;
constexpr int PQ_BYTES = 16 * 8 * 2 * 2 * 14 * 1024;
constexpr int FLAG_OFF = PQ_BYTES;
constexpr int EPOCH_OFF = FLAG_OFF + 256 * 256;
constexpr int WS_BYTES = EPOCH_OFF + 16 * 256;
constexpr int RSRC_W3 = 0x00020000;
constexpr int AUX_SC1 = 16;

#define STAMP(i)

DI unsigned pk2(float a, float b) { bf2 r = __builtin_convertvector(f32x2{a, b}, bf2); return __builtin_bit_cast(unsigned, r); }
template <int AUX = 0> DI void dma16(__amdgpu_buffer_rsrc_t rs, LAS unsigned char* dst, int voff, int soff) {
  __builtin_amdgcn_raw_ptr_buffer_load_lds(rs, (LAS void*)dst, 16, voff, soff, 0, AUX);
}
DI s16x4 trd(LAS unsigned char* p) { return __builtin_amdgcn_ds_read_tr16_b64_v4i16((LAS s16x4*)p); }
DI bf16x8 cat(s16x4 a, s16x4 b) { return __builtin_shufflevector(a, b, 0, 1, 2, 3, 4, 5, 6, 7); }
DI f32x4 mfma(bf16x8 a, bf16x8 b, f32x4 c) { return __builtin_amdgcn_mfma_f32_16x16x32_bf16(a, b, c, 0, 0, 0); }
#define WAITV(n) asm volatile("s_waitcnt vmcnt(" #n ")" ::: "memory")
#define VBAR(n) asm volatile("s_waitcnt vmcnt(" #n ")\n\ts_barrier" ::: "memory")
template <int N> DI void vbar() { asm volatile("s_waitcnt vmcnt(%0)\n\ts_barrier" :: "i"(N) : "memory"); }
#define LBAR() asm volatile("s_waitcnt lgkmcnt(0)\n\ts_barrier" ::: "memory")
#define BAR() asm volatile("s_barrier" ::: "memory")
#define SB() __builtin_amdgcn_sched_barrier(0)
#define SGB(m, n) __builtin_amdgcn_sched_group_barrier(m, n, 0)

#define KORDER(t, mi, j) (((t) + 2 * (mi)) & 15)
DI bf16x8 ldW2(LAS unsigned char* L, int so, int a) { return *(LAS bf16x8*)(L + so + a); }

template <int ND> struct Frags { bf16x8 w[2]; bf16x8 x[ND]; };
template <int ND>
DI void loadFrags(Frags<ND>& f, LAS unsigned char* wA, LAS unsigned char* x0, LAS unsigned char* x1, int ks) {
  constexpr int RP = ND == 2 ? 256 : 128;
  f.x[0] = cat(trd(x0 + (16 * ks) * RP), trd(x0 + (16 * ks + 4) * RP));
  f.w[0] = *(LAS bf16x8*)(wA);
  f.w[1] = *(LAS bf16x8*)(wA + 4096);
  if constexpr (ND == 2) f.x[1] = cat(trd(x1 + (16 * ks) * RP), trd(x1 + (16 * ks + 4) * RP));
}
template <int ND> DI void mma(f32x16 (&acc)[ND][2], const Frags<ND>& f) {
#pragma unroll
  for (int dd = 0; dd < ND; ++dd)
#pragma unroll
    for (int tt = 0; tt < 2; ++tt) acc[dd][tt] = __builtin_amdgcn_mfma_f32_32x32x16_bf16(f.x[dd], f.w[tt], acc[dd][tt], 0, 0, 0);
  if constexpr (ND == 2) { SGB(0x8, 1); SGB(0x100, 3); SGB(0x8, 1); SGB(0x100, 3); SGB(0x8, 2); }
  else { SGB(0x8, 1); SGB(0x100, 4); SGB(0x8, 1); }
  SB();
}
template <int ND>
DI void phaseA_compute(LAS unsigned char* L, int cw, int lane, void* ws, __amdgpu_buffer_rsrc_t rs, int panel, int part, int kh, int j,
                       bf16x8 (&wr)[2][14], const int (&wa)[14]) {
  const int wt = cw & 1, wd = cw >> 1, h = lane >> 5, r32 = lane & 31;
  const int f = (lane >> 1) & 7;
  int wo[4];
#pragma unroll
  for (int ks = 0; ks < 4; ++ks) wo[ks] = (wt * 64 + r32) * 128 + (((2 * ks + h) ^ f) << 4);
  const int blk = (lane >> 4) & 1, q = (lane & 15) >> 2, p = lane & 3;
  int xo0, xo1;
  if constexpr (ND == 2) {
    const int ch = wd * 8 + 2 * blk + (p >> 1);
    xo0 = 16384 + (8 * h + q) * 256 + ((ch ^ (q << 2)) << 4) + 8 * (p & 1);
    xo1 = xo0 ^ 64;
  } else {
    const int ch = wd * 4 + 2 * blk + (p >> 1);
    xo0 = 16384 + (8 * h + q) * 128 + ((ch ^ (((q >> 1) & 1) << 2)) << 4) + 8 * (p & 1);
    xo1 = xo0;
  }
  f32x16 acc[ND][2];
#pragma unroll
  for (int dd = 0; dd < ND; ++dd)
#pragma unroll
    for (int tt = 0; tt < 2; ++tt)
#pragma unroll
      for (int i = 0; i < 16; ++i) acc[dd][tt][i] = 0.f;
  Frags<ND> F0, F1, F2, F3;
  BAR();
  STAMP(1);
  loadFrags<ND>(F0, L + wo[0], L + xo0, L + xo1, 0);
  loadFrags<ND>(F1, L + wo[1], L + xo0, L + xo1, 1);
  SB();
  loadFrags<ND>(F2, L + wo[2], L + xo0, L + xo1, 2);
  mma<ND>(acc, F0);
  loadFrags<ND>(F3, L + wo[3], L + xo0, L + xo1, 3);
  mma<ND>(acc, F1);
  LBAR();
  SB();
#pragma unroll 1
  for (int s = 1; s < 16; ++s) {
    LAS unsigned char* B = L + (s & 3) * SLOT;
    loadFrags<ND>(F0, B + wo[0], B + xo0, B + xo1, 0);
    mma<ND>(acc, F2);
    loadFrags<ND>(F1, B + wo[1], B + xo0, B + xo1, 1);
    mma<ND>(acc, F3);
    loadFrags<ND>(F2, B + wo[2], B + xo0, B + xo1, 2);
    mma<ND>(acc, F0);
    loadFrags<ND>(F3, B + wo[3], B + xo0, B + xo1, 3);
    mma<ND>(acc, F1);
    if (SCHED == 1 || s < 15) LBAR();
    SB();
  }
  STAMP(2);
#pragma unroll
  for (int dd = 0; dd < ND; ++dd)
#pragma unroll
    for (int tt = 0; tt < 2; ++tt) {
      acc[dd][tt] = __builtin_amdgcn_mfma_f32_32x32x16_bf16(F2.x[dd], F2.w[tt], acc[dd][tt], 0, 0, 0);
      acc[dd][tt] = __builtin_amdgcn_mfma_f32_32x32x16_bf16(F3.x[dd], F3.w[tt], acc[dd][tt], 0, 0, 0);
    }
#pragma unroll
  for (int dd = 0; dd < ND; ++dd)
#pragma unroll
    for (int tt = 0; tt < 2; ++tt) {
      const int kap = ND == 2 ? 4 * j + 2 * wd + dd : 12 + wd;
      const int tau = wt * 4 + tt * 2 + (r32 >> 4);
      const int base = ((((panel * 8 + tau) * 2 + part) * 2 + kh) * 14 + kap) * 1024 + (h * 16 + (lane & 15)) * 16;
#pragma unroll
      for (int u = 0; u < 2; ++u) {
        v4u o;
#pragma unroll
        for (int i = 0; i < 4; ++i) o[i] = pk2(acc[dd][tt][8 * u + 2 * i], acc[dd][tt][8 * u + 2 * i + 1]);
        const auto s0 = __builtin_amdgcn_permlane32_swap(o[0], o[2], false, false);
        const auto s1 = __builtin_amdgcn_permlane32_swap(o[1], o[3], false, false);
        __builtin_amdgcn_raw_buffer_store_b128(v4u{s0[0], s1[0], s0[1], s1[1]}, rs, base + u * 512, 0, AUX_SC1);
      }
    }
}

template <int ND>
DI void loader(LAS unsigned char* L, int lw, int lane, const u16* W, const u16* x, const u16* Wd, void* ws, int panel, int kh, int j, int mi) {
  const __amdgpu_buffer_rsrc_t rw = __builtin_amdgcn_make_buffer_rsrc((void*)W, 0, SEQ * SEQ * 2, RSRC_W3);
  const __amdgpu_buffer_rsrc_t rx = __builtin_amdgcn_make_buffer_rsrc((void*)x, 0, SEQ * DH * 2, RSRC_W3);
  const __amdgpu_buffer_rsrc_t rd = __builtin_amdgcn_make_buffer_rsrc((void*)Wd, 0, DH * DH * 2, RSRC_W3);
  const int sz = (((lane & 7) ^ ((4 * (lw & 1) + (lane >> 4)) & 7)) << 4);
  const int vw = (lane >> 3) * 4096 + sz, vd = (lane >> 3) * 128 + sz;
  int vx;
  vx = ND == 2 ? (lane >> 4) * 896 + (((lane & 15) ^ ((lane >> 4) << 2)) << 4) : (lane >> 3) * 896 + (((lane & 7) ^ (((lane >> 4) & 1) << 2)) << 4);
  const int sw0 = (panel * 128 + 8 * lw) * 4096 + kh * 2048;
  const int sx0 = (kh * 1024 + (ND == 2 ? 4 : 8) * lw) * 896 + j * 256;
  const int sd0 = j * 112 * 896 + (lw & 1) * 1024;
  int et = mi == 7 ? 0 : mi;
  constexpr int a = 4 + 2 * ND, c = 7;
  auto A = [&](int n) {
    const int kst = KORDER(n, mi, j);
    LAS unsigned char* B = L + (n & 3) * SLOT + lw * 1024;
    const int sw = sw0 + kst * 128, sx = sx0 + kst * 64 * 896;
#pragma unroll
    for (int q = 0; q < 4; ++q) dma16<AUXW>(rw, B + q * 4096, vw, sw + q * 32 * 4096);
#pragma unroll
    for (int q = 0; q < 2 * ND; ++q) dma16(rx, B + 16384 + q * 4096, vx, sx + q * (ND == 2 ? 16 : 32) * 896);
  };
  auto Q = [&](int so) {
    LAS unsigned char* B = L + so + (lw >> 1) * 14336 + (lw & 1) * 1024;
    const int sd = sd0 + et * 14336;
#pragma unroll
    for (int i = 0; i < 7; ++i) dma16(rd, B + i * 2048, vd, sd + i * 2048);
    et = et == 6 ? 0 : et + 1;
  };
#pragma unroll 1
  for (int n = 0; n < 16; ++n) {
    if (n == 2 || n == 3) vbar<a>();
    else if ((unsigned)(n - E0 - 1) < 3u) vbar<2 * a + c>();
    else if (n >= 5) vbar<2 * a>();
    A(n);
    if (n == E0) Q(XSLOT);
  }
  vbar<2 * a>();
  vbar<a>();
  vbar<0>(); Q(0); Q(SLOT);
  vbar<c>(); Q(2 * SLOT); Q(3 * SLOT);
  BAR(); Q(0); Q(XSLOT);
}

__global__ __launch_bounds__(512, 2) void fnet(const u16* __restrict__ x, const u16* __restrict__ sre, const u16* __restrict__ sim,
                                               const u16* __restrict__ dre, const u16* __restrict__ dim, void* ws, u16* __restrict__ y) {
  __shared__ __attribute__((aligned(16))) unsigned char smem[LDS_BYTES];
  LAS unsigned char* L = (LAS unsigned char*)smem;
  const int lane = threadIdx.x & 63, w = __builtin_amdgcn_readfirstlane(threadIdx.x >> 6);
  const int b = blockIdx.x, xcd = b & 7, r = b >> 3;
  const int kh = xcd & 1, mi = r >> 2, j = r & 3;
  const int panel = (xcd >> 1) * 4 + (mi >> 1), part = mi & 1;
  const int mb = kh * 2 + part, nb = j;
  STAMP(0);
  if (w >= 4) {
    const int lw = w - 4;
    const u16* W = part ? sim : sre;
    const u16* Wd = (lw >> 1) ? dim : dre;
    if (j < 3) loader<2>(L, lw, lane, W, x, Wd, ws, panel, kh, j, mi);
    else loader<1>(L, lw, lane, W, x, Wd, ws, panel, kh, j, mi);
    WAITV(0);
    STAMP(2);
    BAR();
    BAR();
  } else {
    const int cw = w;
    const __amdgpu_buffer_rsrc_t rs = __builtin_amdgcn_make_buffer_rsrc(ws, 0, WS_BYTES, RSRC_W3);
    const v2u ep = __builtin_amdgcn_raw_buffer_load_b64(rs, 0, EPOCH_OFF + panel * 256, AUX_SC1);
    const int pb = cw >> 1, ti = cw & 1;
    const int g = lane >> 4, e = lane & 15;
    const int krot = (nb * 7) >> 1;
    const int erot = mi == 7 ? 0 : mi;
    int wa[14];
#pragma unroll
    for (int i = 0; i < 14; ++i) {
      int kap = i + krot; kap = kap >= 14 ? kap - 14 : kap;
      const int Ln = 7 * e + (kap >> 1), c = 4 * (kap & 1) + g;
      wa[i] = pb * 14336 + Ln * 128 + ((c ^ ((Ln >> 1) & 7)) << 4);
      asm volatile("" : "+v"(wa[i]));
    }
    bf16x8 wr[2][14];
    const u64 tag = ((u64)__builtin_amdgcn_readfirstlane(ep.y) << 32 | __builtin_amdgcn_readfirstlane(ep.x)) + 1;
    if (j < 3) phaseA_compute<2>(L, cw, lane, ws, rs, panel, part, kh, j, wr, wa);
    else phaseA_compute<1>(L, cw, lane, ws, rs, panel, part, kh, j, wr, wa);
    LBAR();
    STAMP(3);
#pragma unroll
    for (int i = 0; i < 14; ++i) { wr[0][i] = ldW2(L, XSLOT, wa[i]); wr[1][i] = ldW2(L, 0, wa[i]); }
    WAITV(0);
    STAMP(5);
    {
      const int wt = cw & 1, wd = cw >> 1;
      const int cmb = 2 * wt + ((lane >> 2) & 1), cnb = lane & 3;
      const int cons = (panel * 4 + cmb) * 4 + cnb;
      const int ent = (part * 2 + kh) * 8 + 2 * j + wd;
      if (lane < 8) __builtin_amdgcn_raw_buffer_store_b64(v2u{(unsigned)tag, (unsigned)(tag >> 32)}, rs, FLAG_OFF + cons * 256 + ent * 8, 0, AUX_SC1);
    }
    LBAR();
    BAR();
    bf16x8 wf[2][5];
    constexpr int so[5] = {SLOT, 2 * SLOT, 3 * SLOT, 0, XSLOT};
#pragma unroll
    for (int q = 0; q < 5; ++q) wf[0][q] = ldW2(L, so[q], wa[0]);
    {
      const int cons = (panel * 4 + mb) * 4 + nb;
      unsigned spins = 0;
      for (;;) {
        const v2u v = __builtin_amdgcn_raw_buffer_load_b64(rs, (lane & 15) * 8, FLAG_OFF + cons * 256 + pb * 128, AUX_SC1);
        const bool ok = v.x == (unsigned)tag && v.y == (unsigned)(tag >> 32);
        if (__all(ok) || ++spins > (1u << 16)) break;
        __builtin_amdgcn_s_sleep(1);
      }
      __builtin_amdgcn_fence(__ATOMIC_ACQUIRE, "wavefront");
    }
    STAMP(6);
    constexpr int PD = 4;
    v4u pq[14][2];
    const int pqb = (((panel * 8 + mb * 2 + ti) * 2 + pb) * 2) * 14 * 1024;
    auto ldpq = [&](int i) {
      int kap = i + krot; kap = kap >= 14 ? kap - 14 : kap;
#pragma unroll
      for (int h2 = 0; h2 < 2; ++h2) pq[i][h2] = __builtin_amdgcn_raw_buffer_load_b128(rs, lane * 16, pqb + (h2 * 14 + kap) * 1024, AUX_SC1);
    };
#pragma unroll
    for (int i = 0; i < PD; ++i) ldpq(i);
    SB();
    f32x4 acc[7];
#pragma unroll
    for (int q = 0; q < 7; ++q) acc[q] = f32x4{0, 0, 0, 0};
#pragma unroll
    for (int i = 0; i < 14; ++i) {
      if (i + PD < 14) ldpq(i + PD);
      if (i + 1 < 14) {
#pragma unroll
        for (int q = 0; q < 5; ++q) wf[(i + 1) & 1][q] = ldW2(L, so[q], wa[i + 1]);
      }
#pragma unroll
      for (int h2 = 0; h2 < 2; ++h2)
#pragma unroll
        for (int q = 0; q < 7; ++q) acc[q] = mfma(q < 2 ? wr[q][i] : wf[i & 1][q - 2], __builtin_bit_cast(bf16x8, pq[i][h2]), acc[q]);
      SGB(0x20, 2);
#pragma unroll
      for (int q = 0; q < 5; ++q) { SGB(0x8, 1); SGB(0x100, 1); }
      SGB(0x8, 9);
      SB();
    }
    STAMP(7);
    LAS unsigned char* X = L + 28672 + lane * 16;
#pragma unroll
    for (int q = 0; q < 7; ++q) {
      const int t = ti * 7 + q;
      if ((q < 4) == (pb == 1)) *(LAS f32x4*)(X + (t >> 2) * SLOT + (t & 3) * 1024) = acc[q];
    }
    LBAR();
    if (threadIdx.x == 0 && mb == 0 && nb == 0)
      __builtin_amdgcn_raw_buffer_store_b64(v2u{(unsigned)tag, (unsigned)(tag >> 32)}, rs, EPOCH_OFF + panel * 256, 0, AUX_SC1);
    u16* yrow = y + (size_t)(panel * 128 + mb * 32 + ti * 16 + e) * DH + nb * 112;
    auto fin = [&](int q) { const int t = ti * 7 + q; const f32x4 o = *(LAS f32x4*)(X + (t >> 2) * SLOT + (t & 3) * 1024); return q < 4 ? acc[q] - o : o - acc[q]; };
    auto st2 = [&](int qa, int qb) {
      const f32x4 a = fin(qa), c = fin(qb);
      const auto s0 = __builtin_amdgcn_permlane16_swap(pk2(a[0], a[1]), pk2(c[0], c[1]), false, false);
      const auto s1 = __builtin_amdgcn_permlane16_swap(pk2(a[2], a[3]), pk2(c[2], c[3]), false, false);
      int et = ((g & 1) ? qb : qa) + erot; et = et >= 7 ? et - 7 : et;
      *(GAS v4u*)(yrow + 16 * et + 8 * (g >> 1)) = v4u{s0[0], s1[0], s0[1], s1[1]};
    };
    if (pb == 0) { st2(0, 1); st2(2, 3); }
    else {
      st2(4, 5);
      const f32x4 a = fin(6);
      int et = 6 + erot; et = et >= 7 ? et - 7 : et;
      *(GAS v2u*)(yrow + 16 * et + 4 * g) = v2u{pk2(a[0], a[1]), pk2(a[2], a[3])};
    }
  }
  STAMP(8);
}

extern "C" void kernel_launch(void* const* d_in, const int* in_sizes, int n_in, void* d_out, int out_size, void* d_ws, size_t ws_size,
                              hipStream_t stream) {
  fnet<<<256, 512, 0, stream>>>((const u16*)d_in[0], (const u16*)d_in[1], (const u16*)d_in[2], (const u16*)d_in[3], (const u16*)d_in[4], d_ws, (u16*)d_out);
}
```

```cpp
#define AUXY 16
#include <hip/hip_runtime.h>
#include <hip/hip_bf16.h>
typedef unsigned short u16;
typedef unsigned long long u64;
typedef short s16x4 __attribute__((ext_vector_type(4)));
typedef short bf16x8 __attribute__((ext_vector_type(8)));
typedef float f32x4 __attribute__((ext_vector_type(4)));
typedef float f32x16 __attribute__((ext_vector_type(16)));
typedef unsigned v4u __attribute__((ext_vector_type(4)));
typedef unsigned v2u __attribute__((ext_vector_type(2)));
typedef __bf16 bf2 __attribute__((ext_vector_type(2)));
typedef float f32x2 __attribute__((ext_vector_type(2)));
#define GAS __attribute__((address_space(1)))
#define LAS __attribute__((address_space(3)))
#define DI __device__ __forceinline__

#define AUXW 0
#define SCHED 2
#define SHAPE16 0
#define E0 4

constexpr int SEQ = 2048, DH = 448;
constexpr int SLOT = 32768;
constexpr int XSLOT = 4 * SLOT;
constexpr int LDS_BYTES = 163840;
constexpr int PQ_BYTES = 16 * 8 * 2 * 2 * 14 * 1024;
constexpr int FLAG_OFF = PQ_BYTES;
constexpr int EPOCH_OFF = FLAG_OFF + 256 * 256;
constexpr int WS_BYTES = EPOCH_OFF + 16 * 256;
constexpr int RSRC_W3 = 0x00020000;
constexpr int AUX_SC1 = 16;

#define STAMP(i)

DI unsigned pk2(float a, float b) { bf2 r = __builtin_convertvector(f32x2{a, b}, bf2); return __builtin_bit_cast(unsigned, r); }
template <int AUX = 0> DI void dma16(__amdgpu_buffer_rsrc_t rs, LAS unsigned char* dst, int voff, int soff) {
  __builtin_amdgcn_raw_ptr_buffer_load_lds(rs, (LAS void*)dst, 16, voff, soff, 0, AUX);
}
DI s16x4 trd(LAS unsigned char* p) { return __builtin_amdgcn_ds_read_tr16_b64_v4i16((LAS s16x4*)p); }
DI bf16x8 cat(s16x4 a, s16x4 b) { return __builtin_shufflevector(a, b, 0, 1, 2, 3, 4, 5, 6, 7); }
DI f32x4 mfma(bf16x8 a, bf16x8 b, f32x4 c) { return __builtin_amdgcn_mfma_f32_16x16x32_bf16(a, b, c, 0, 0, 0); }
#define WAITV(n) asm volatile("s_waitcnt vmcnt(" #n ")" ::: "memory")
#define VBAR(n) asm volatile("s_waitcnt vmcnt(" #n ")\n\ts_barrier" ::: "memory")
template <int N> DI void vbar() { asm volatile("s_waitcnt vmcnt(%0)\n\ts_barrier" :: "i"(N) : "memory"); }
#define LBAR() asm volatile("s_waitcnt lgkmcnt(0)\n\ts_barrier" ::: "memory")
#define BAR() asm volatile("s_barrier" ::: "memory")
#define SB() __builtin_amdgcn_sched_barrier(0)
#define SGB(m, n) __builtin_amdgcn_sched_group_barrier(m, n, 0)

#define KORDER(t, mi, j) (((t) + 2 * (mi)) & 15)
DI bf16x8 ldW2(LAS unsigned char* L, int so, int a) { return *(LAS bf16x8*)(L + so + a); }

template <int ND> struct Frags { bf16x8 w[2]; bf16x8 x[ND]; };
template <int ND>
DI void loadFrags(Frags<ND>& f, LAS unsigned char* wA, LAS unsigned char* x0, LAS unsigned char* x1, int ks) {
  constexpr int RP = ND == 2 ? 256 : 128;
  f.x[0] = cat(trd(x0 + (16 * ks) * RP), trd(x0 + (16 * ks + 4) * RP));
  f.w[0] = *(LAS bf16x8*)(wA);
  f.w[1] = *(LAS bf16x8*)(wA + 4096);
  if constexpr (ND == 2) f.x[1] = cat(trd(x1 + (16 * ks) * RP), trd(x1 + (16 * ks + 4) * RP));
}
template <int ND> DI void mma(f32x16 (&acc)[ND][2], const Frags<ND>& f) {
#pragma unroll
  for (int dd = 0; dd < ND; ++dd)
#pragma unroll
    for (int tt = 0; tt < 2; ++tt) acc[dd][tt] = __builtin_amdgcn_mfma_f32_32x32x16_bf16(f.x[dd], f.w[tt], acc[dd][tt], 0, 0, 0);
  if constexpr (ND == 2) { SGB(0x8, 1); SGB(0x100, 3); SGB(0x8, 1); SGB(0x100, 3); SGB(0x8, 2); }
  else { SGB(0x8, 1); SGB(0x100, 4); SGB(0x8, 1); }
  SB();
}
template <int ND>
DI void phaseA_compute(LAS unsigned char* L, int cw, int lane, void* ws, __amdgpu_buffer_rsrc_t rs, int panel, int part, int kh, int j,
                       bf16x8 (&wr)[2][14], const int (&wa)[14]) {
  const int wt = cw & 1, wd = cw >> 1, h = lane >> 5, r32 = lane & 31;
  const int f = (lane >> 1) & 7;
  int wo[4];
#pragma unroll
  for (int ks = 0; ks < 4; ++ks) wo[ks] = (wt * 64 + r32) * 128 + (((2 * ks + h) ^ f) << 4);
  const int blk = (lane >> 4) & 1, q = (lane & 15) >> 2, p = lane & 3;
  int xo0, xo1;
  if constexpr (ND == 2) {
    const int ch = wd * 8 + 2 * blk + (p >> 1);
    xo0 = 16384 + (8 * h + q) * 256 + ((ch ^ (q << 2)) << 4) + 8 * (p & 1);
    xo1 = xo0 ^ 64;
  } else {
    const int ch = wd * 4 + 2 * blk + (p >> 1);
    xo0 = 16384 + (8 * h + q) * 128 + ((ch ^ (((q >> 1) & 1) << 2)) << 4) + 8 * (p & 1);
    xo1 = xo0;
  }
  f32x16 acc[ND][2];
#pragma unroll
  for (int dd = 0; dd < ND; ++dd)
#pragma unroll
    for (int tt = 0; tt < 2; ++tt)
#pragma unroll
      for (int i = 0; i < 16; ++i) acc[dd][tt][i] = 0.f;
  Frags<ND> F0, F1, F2, F3;
  BAR();
  STAMP(1);
  loadFrags<ND>(F0, L + wo[0], L + xo0, L + xo1, 0);
  loadFrags<ND>(F1, L + wo[1], L + xo0, L + xo1, 1);
  SB();
  loadFrags<ND>(F2, L + wo[2], L + xo0, L + xo1, 2);
  mma<ND>(acc, F0);
  loadFrags<ND>(F3, L + wo[3], L + xo0, L + xo1, 3);
  mma<ND>(acc, F1);
  LBAR();
  SB();
#pragma unroll 1
  for (int s = 1; s < 16; ++s) {
    LAS unsigned char* B = L + (s & 3) * SLOT;
    loadFrags<ND>(F0, B + wo[0], B + xo0, B + xo1, 0);
    mma<ND>(acc, F2);
    loadFrags<ND>(F1, B + wo[1], B + xo0, B + xo1, 1);
    mma<ND>(acc, F3);
    loadFrags<ND>(F2, B + wo[2], B + xo0, B + xo1, 2);
    mma<ND>(acc, F0);
    loadFrags<ND>(F3, B + wo[3], B + xo0, B + xo1, 3);
    mma<ND>(acc, F1);
    if (SCHED == 1 || s < 15) LBAR();
    SB();
  }
  STAMP(2);
#pragma unroll
  for (int dd = 0; dd < ND; ++dd)
#pragma unroll
    for (int tt = 0; tt < 2; ++tt) {
      acc[dd][tt] = __builtin_amdgcn_mfma_f32_32x32x16_bf16(F2.x[dd], F2.w[tt], acc[dd][tt], 0, 0, 0);
      acc[dd][tt] = __builtin_amdgcn_mfma_f32_32x32x16_bf16(F3.x[dd], F3.w[tt], acc[dd][tt], 0, 0, 0);
    }
#pragma unroll
  for (int dd = 0; dd < ND; ++dd)
#pragma unroll
    for (int tt = 0; tt < 2; ++tt) {
      const int kap = ND == 2 ? 4 * j + 2 * wd + dd : 12 + wd;
      const int tau = wt * 4 + tt * 2 + (r32 >> 4);
      const int base = ((((panel * 8 + tau) * 2 + part) * 2 + kh) * 14 + kap) * 1024 + (h * 16 + (lane & 15)) * 16;
#pragma unroll
      for (int u = 0; u < 2; ++u) {
        v4u o;
#pragma unroll
        for (int i = 0; i < 4; ++i) o[i] = pk2(acc[dd][tt][8 * u + 2 * i], acc[dd][tt][8 * u + 2 * i + 1]);
        const auto s0 = __builtin_amdgcn_permlane32_swap(o[0], o[2], false, false);
        const auto s1 = __builtin_amdgcn_permlane32_swap(o[1], o[3], false, false);
        __builtin_amdgcn_raw_buffer_store_b128(v4u{s0[0], s1[0], s0[1], s1[1]}, rs, base + u * 512, 0, AUX_SC1);
      }
    }
}

template <int ND>
DI void loader(LAS unsigned char* L, int lw, int lane, const u16* W, const u16* x, const u16* Wd, void* ws, int panel, int kh, int j, int mi) {
  const __amdgpu_buffer_rsrc_t rw = __builtin_amdgcn_make_buffer_rsrc((void*)W, 0, SEQ * SEQ * 2, RSRC_W3);
  const __amdgpu_buffer_rsrc_t rx = __builtin_amdgcn_make_buffer_rsrc((void*)x, 0, SEQ * DH * 2, RSRC_W3);
  const __amdgpu_buffer_rsrc_t rd = __builtin_amdgcn_make_buffer_rsrc((void*)Wd, 0, DH * DH * 2, RSRC_W3);
  const int sz = (((lane & 7) ^ ((4 * (lw & 1) + (lane >> 4)) & 7)) << 4);
  const int vw = (lane >> 3) * 4096 + sz, vd = (lane >> 3) * 128 + sz;
  int vx;
  vx = ND == 2 ? (lane >> 4) * 896 + (((lane & 15) ^ ((lane >> 4) << 2)) << 4) : (lane >> 3) * 896 + (((lane & 7) ^ (((lane >> 4) & 1) << 2)) << 4);
  const int sw0 = (panel * 128 + 8 * lw) * 4096 + kh * 2048;
  const int sx0 = (kh * 1024 + (ND == 2 ? 4 : 8) * lw) * 896 + j * 256;
  const int sd0 = j * 112 * 896 + (lw & 1) * 1024;
  int et = mi == 7 ? 0 : mi;
  constexpr int a = 4 + 2 * ND, c = 7;
  auto A = [&](int n) {
    const int kst = KORDER(n, mi, j);
    LAS unsigned char* B = L + (n & 3) * SLOT + lw * 1024;
    const int sw = sw0 + kst * 128, sx = sx0 + kst * 64 * 896;
#pragma unroll
    for (int q = 0; q < 4; ++q) dma16<AUXW>(rw, B + q * 4096, vw, sw + q * 32 * 4096);
#pragma unroll
    for (int q = 0; q < 2 * ND; ++q) dma16(rx, B + 16384 + q * 4096, vx, sx + q * (ND == 2 ? 16 : 32) * 896);
  };
  auto Q = [&](int so) {
    LAS unsigned char* B = L + so + (lw >> 1) * 14336 + (lw & 1) * 1024;
    const int sd = sd0 + et * 14336;
#pragma unroll
    for (int i = 0; i < 7; ++i) dma16(rd, B + i * 2048, vd, sd + i * 2048);
    et = et == 6 ? 0 : et + 1;
  };
#pragma unroll 1
  for (int n = 0; n < 16; ++n) {
    if (n == 2 || n == 3) vbar<a>();
    else if ((unsigned)(n - E0 - 1) < 3u) vbar<2 * a + c>();
    else if (n >= 5) vbar<2 * a>();
    A(n);
    if (n == E0) Q(XSLOT);
  }
  vbar<2 * a>();
  vbar<a>();
  vbar<0>(); Q(0); Q(SLOT);
  vbar<c>(); Q(2 * SLOT); Q(3 * SLOT);
  BAR(); Q(0); Q(XSLOT);
}

__global__ __launch_bounds__(512, 2) void fnet(const u16* __restrict__ x, const u16* __restrict__ sre, const u16* __restrict__ sim,
                                               const u16* __restrict__ dre, const u16* __restrict__ dim, void* ws, u16* __restrict__ y) {
  __shared__ __attribute__((aligned(16))) unsigned char smem[LDS_BYTES];
  LAS unsigned char* L = (LAS unsigned char*)smem;
  const int lane = threadIdx.x & 63, w = __builtin_amdgcn_readfirstlane(threadIdx.x >> 6);
  const int b = blockIdx.x, xcd = b & 7, r = b >> 3;
  const int kh = xcd & 1, mi = r >> 2, j = r & 3;
  const int panel = (xcd >> 1) * 4 + (mi >> 1), part = mi & 1;
  const int mb = kh * 2 + part, nb = j;
  STAMP(0);
  if (w >= 4) {
    const int lw = w - 4;
    const u16* W = part ? sim : sre;
    const u16* Wd = (lw >> 1) ? dim : dre;
    if (j < 3) loader<2>(L, lw, lane, W, x, Wd, ws, panel, kh, j, mi);
    else loader<1>(L, lw, lane, W, x, Wd, ws, panel, kh, j, mi);
    WAITV(0);
    STAMP(2);
    BAR();
    BAR();
  } else {
    const int cw = w;
    const __amdgpu_buffer_rsrc_t rs = __builtin_amdgcn_make_buffer_rsrc(ws, 0, WS_BYTES, RSRC_W3);
    const v2u ep = __builtin_amdgcn_raw_buffer_load_b64(rs, 0, EPOCH_OFF + panel * 256, AUX_SC1);
    const int pb = cw >> 1, ti = cw & 1;
    const int g = lane >> 4, e = lane & 15;
    const int krot = (nb * 7) >> 1;
    const int erot = mi == 7 ? 0 : mi;
    int wa[14];
#pragma unroll
    for (int i = 0; i < 14; ++i) {
      int kap = i + krot; kap = kap >= 14 ? kap - 14 : kap;
      const int Ln = 7 * e + (kap >> 1), c = 4 * (kap & 1) + g;
      wa[i] = pb * 14336 + Ln * 128 + ((c ^ ((Ln >> 1) & 7)) << 4);
      asm volatile("" : "+v"(wa[i]));
    }
    bf16x8 wr[2][14];
    const u64 tag = ((u64)__builtin_amdgcn_readfirstlane(ep.y) << 32 | __builtin_amdgcn_readfirstlane(ep.x)) + 1;
    if (j < 3) phaseA_compute<2>(L, cw, lane, ws, rs, panel, part, kh, j, wr, wa);
    else phaseA_compute<1>(L, cw, lane, ws, rs, panel, part, kh, j, wr, wa);
    LBAR();
    STAMP(3);
#pragma unroll
    for (int i = 0; i < 14; ++i) { wr[0][i] = ldW2(L, XSLOT, wa[i]); wr[1][i] = ldW2(L, 0, wa[i]); }
    WAITV(0);
    STAMP(5);
    {
      const int wt = cw & 1, wd = cw >> 1;
      const int cmb = 2 * wt + ((lane >> 2) & 1), cnb = lane & 3;
      const int cons = (panel * 4 + cmb) * 4 + cnb;
      const int ent = (part * 2 + kh) * 8 + 2 * j + wd;
      if (lane < 8) __builtin_amdgcn_raw_buffer_store_b64(v2u{(unsigned)tag, (unsigned)(tag >> 32)}, rs, FLAG_OFF + cons * 256 + ent * 8, 0, AUX_SC1);
    }
    LBAR();
    BAR();
    bf16x8 wf[2][5];
    constexpr int so[5] = {SLOT, 2 * SLOT, 3 * SLOT, 0, XSLOT};
#pragma unroll
    for (int q = 0; q < 5; ++q) wf[0][q] = ldW2(L, so[q], wa[0]);
    {
      const int cons = (panel * 4 + mb) * 4 + nb;
      unsigned spins = 0;
      for (;;) {
        const v2u v = __builtin_amdgcn_raw_buffer_load_b64(rs, (lane & 15) * 8, FLAG_OFF + cons * 256 + pb * 128, AUX_SC1);
        const bool ok = v.x == (unsigned)tag && v.y == (unsigned)(tag >> 32);
        if (__all(ok) || ++spins > (1u << 16)) break;
        __builtin_amdgcn_s_sleep(1);
      }
      __builtin_amdgcn_fence(__ATOMIC_ACQUIRE, "wavefront");
    }
    STAMP(6);
    constexpr int PD = 4;
    v4u pq[14][2];
    const int pqb = (((panel * 8 + mb * 2 + ti) * 2 + pb) * 2) * 14 * 1024;
    auto ldpq = [&](int i) {
      int kap = i + krot; kap = kap >= 14 ? kap - 14 : kap;
#pragma unroll
      for (int h2 = 0; h2 < 2; ++h2) pq[i][h2] = __builtin_amdgcn_raw_buffer_load_b128(rs, lane * 16, pqb + (h2 * 14 + kap) * 1024, AUX_SC1);
    };
#pragma unroll
    for (int i = 0; i < PD; ++i) ldpq(i);
    SB();
    f32x4 acc[7];
#pragma unroll
    for (int q = 0; q < 7; ++q) acc[q] = f32x4{0, 0, 0, 0};
#pragma unroll
    for (int i = 0; i < 14; ++i) {
      if (i + PD < 14) ldpq(i + PD);
      if (i + 1 < 14) {
#pragma unroll
        for (int q = 0; q < 5; ++q) wf[(i + 1) & 1][q] = ldW2(L, so[q], wa[i + 1]);
      }
#pragma unroll
      for (int h2 = 0; h2 < 2; ++h2)
#pragma unroll
        for (int q = 0; q < 7; ++q) acc[q] = mfma(q < 2 ? wr[q][i] : wf[i & 1][q - 2], __builtin_bit_cast(bf16x8, pq[i][h2]), acc[q]);
      SGB(0x20, 2);
#pragma unroll
      for (int q = 0; q < 5; ++q) { SGB(0x8, 1); SGB(0x100, 1); }
      SGB(0x8, 9);
      SB();
    }
    STAMP(7);
    LAS unsigned char* X = L + 28672 + lane * 16;
#pragma unroll
    for (int q = 0; q < 7; ++q) {
      const int t = ti * 7 + q;
      if ((q < 4) == (pb == 1)) *(LAS f32x4*)(X + (t >> 2) * SLOT + (t & 3) * 1024) = acc[q];
    }
    LBAR();
    if (threadIdx.x == 0 && mb == 0 && nb == 0)
      __builtin_amdgcn_raw_buffer_store_b64(v2u{(unsigned)tag, (unsigned)(tag >> 32)}, rs, EPOCH_OFF + panel * 256, 0, AUX_SC1);
    const __amdgpu_buffer_rsrc_t ry = __builtin_amdgcn_make_buffer_rsrc((void*)y, 0, SEQ * DH * 2, RSRC_W3);
    const int yrow = ((panel * 128 + mb * 32 + ti * 16 + e) * DH + nb * 112) * 2;
    auto fin = [&](int q) { const int t = ti * 7 + q; const f32x4 o = *(LAS f32x4*)(X + (t >> 2) * SLOT + (t & 3) * 1024); return q < 4 ? acc[q] - o : o - acc[q]; };
    auto st2 = [&](int qa, int qb) {
      const f32x4 a = fin(qa), c = fin(qb);
      const auto s0 = __builtin_amdgcn_permlane16_swap(pk2(a[0], a[1]), pk2(c[0], c[1]), false, false);
      const auto s1 = __builtin_amdgcn_permlane16_swap(pk2(a[2], a[3]), pk2(c[2], c[3]), false, false);
      int et = ((g & 1) ? qb : qa) + erot; et = et >= 7 ? et - 7 : et;
      __builtin_amdgcn_raw_buffer_store_b128(v4u{s0[0], s1[0], s0[1], s1[1]}, ry, yrow + 32 * et + 16 * (g >> 1), 0, AUXY);
    };
    if (pb == 0) { st2(0, 1); st2(2, 3); }
    else {
      st2(4, 5);
      const f32x4 a = fin(6);
      int et = 6 + erot; et = et >= 7 ? et - 7 : et;
      __builtin_amdgcn_raw_buffer_store_b64(v2u{pk2(a[0], a[1]), pk2(a[2], a[3])}, ry, yrow + 32 * et + 8 * g, 0, AUXY);
    }
  }
  STAMP(8);
}

extern "C" void kernel_launch(void* const* d_in, const int* in_sizes, int n_in, void* d_out, int out_size, void* d_ws, size_t ws_size,
                              hipStream_t stream) {
  fnet<<<256, 512, 0, stream>>>((const u16*)d_in[0], (const u16*)d_in[1], (const u16*)d_in[2], (const u16*)d_in[3], (const u16*)d_in[4], d_ws, (u16*)d_out);
}
```

```cpp
#define STAMPS
#define PROBE
#include <hip/hip_runtime.h>
typedef unsigned short u16;
typedef unsigned long long u64;
typedef short s16x4 __attribute__((ext_vector_type(4)));
typedef short bf16x8 __attribute__((ext_vector_type(8)));
typedef float f32x4 __attribute__((ext_vector_type(4)));
typedef float f32x16 __attribute__((ext_vector_type(16)));
typedef unsigned v4u __attribute__((ext_vector_type(4)));
typedef unsigned v2u __attribute__((ext_vector_type(2)));
typedef __bf16 bf2 __attribute__((ext_vector_type(2)));
typedef float f32x2 __attribute__((ext_vector_type(2)));
#define GAS __attribute__((address_space(1)))
#define LAS __attribute__((address_space(3)))
#define DI __device__ __forceinline__

constexpr int SEQ = 2048, DH = 448;
constexpr int SLOT = 32768;
constexpr int XSLOT = 4 * SLOT;
constexpr int LDS_BYTES = 163840;
constexpr int PQ_BYTES = 16 * 8 * 2 * 2 * 14 * 1024;
constexpr int FLAG_OFF = PQ_BYTES;
constexpr int EPOCH_OFF = FLAG_OFF + 1024 * 128;
constexpr int WS_BYTES = EPOCH_OFF + 16 * 256;
constexpr int RSRC_W3 = 0x00020000;
constexpr int AUX_SC1 = 16;

constexpr size_t STAMP_OFF = 64u << 20;
#define STAMP_ON(i) true
#define STAMP(i) do { if (STAMP_ON(i) && (threadIdx.x & 63) == 0) { unsigned long long* sp_ = (unsigned long long*)((char*)ws + STAMP_OFF) + ((size_t)blockIdx.x * 8 + (threadIdx.x >> 6)) * 32; \
   sp_[2 * (i)] = __builtin_amdgcn_s_memrealtime(); sp_[2 * (i) + 1] = __builtin_amdgcn_s_memtime(); } } while (0)

DI unsigned pk2(float a, float b) { bf2 r = __builtin_convertvector(f32x2{a, b}, bf2); return __builtin_bit_cast(unsigned, r); }
DI void dma16(__amdgpu_buffer_rsrc_t rs, LAS unsigned char* dst, int voff, int soff) {
  __builtin_amdgcn_raw_ptr_buffer_load_lds(rs, (LAS void*)dst, 16, voff, soff, 0, 0);
}
DI s16x4 trd(LAS unsigned char* p) { return __builtin_amdgcn_ds_read_tr16_b64_v4i16((LAS s16x4*)p); }
DI bf16x8 cat(s16x4 a, s16x4 b) { return __builtin_shufflevector(a, b, 0, 1, 2, 3, 4, 5, 6, 7); }
DI f32x4 mfma16(bf16x8 a, bf16x8 b, f32x4 c) { return __builtin_amdgcn_mfma_f32_16x16x32_bf16(a, b, c, 0, 0, 0); }
DI f32x16 mfma32(bf16x8 a, bf16x8 b, f32x16 c) { return __builtin_amdgcn_mfma_f32_32x32x16_bf16(a, b, c, 0, 0, 0); }
template <int N> DI void vbar() { asm volatile("s_waitcnt vmcnt(%0)\n\ts_barrier" :: "i"(N) : "memory"); }
#define WAITV0() asm volatile("s_waitcnt vmcnt(0)" ::: "memory")
#define LBAR() asm volatile("s_waitcnt lgkmcnt(0)\n\ts_barrier" ::: "memory")
#define BAR() asm volatile("s_barrier" ::: "memory")
#define SB() __builtin_amdgcn_sched_barrier(0)
#define SGB(m, n) __builtin_amdgcn_sched_group_barrier(m, n, 0)
DI bf16x8 ldW2(LAS unsigned char* L, int so, int a) { return *(LAS bf16x8*)(L + so + a); }

template <int ND> struct Frags { bf16x8 w[2]; bf16x8 x[ND]; };
template <int ND>
DI void loadFrags(Frags<ND>& f, LAS unsigned char* wA, LAS unsigned char* x0, LAS unsigned char* x1, int ks) {
  constexpr int RP = ND == 2 ? 256 : 128;
  f.x[0] = cat(trd(x0 + (16 * ks) * RP), trd(x0 + (16 * ks + 4) * RP));
  f.w[0] = *(LAS bf16x8*)(wA);
  f.w[1] = *(LAS bf16x8*)(wA + 4096);
  if constexpr (ND == 2) f.x[1] = cat(trd(x1 + (16 * ks) * RP), trd(x1 + (16 * ks + 4) * RP));
}
template <int ND> DI void mma(f32x16 (&acc)[ND][2], const Frags<ND>& f) {
#pragma unroll
  for (int dd = 0; dd < ND; ++dd)
#pragma unroll
    for (int tt = 0; tt < 2; ++tt) acc[dd][tt] = mfma32(f.x[dd], f.w[tt], acc[dd][tt]);
  if constexpr (ND == 2) { SGB(0x8, 1); SGB(0x100, 3); SGB(0x8, 1); SGB(0x100, 3); SGB(0x8, 2); }
  else { SGB(0x8, 1); SGB(0x100, 4); SGB(0x8, 1); }
  SB();
}

template <int ND>
DI void phaseA_compute(LAS unsigned char* L, int cw, int lane, void* ws, __amdgpu_buffer_rsrc_t rs, int panel, int part, int kh, int j) {
  const int wt = cw & 1, wd = cw >> 1, h = lane >> 5, r32 = lane & 31;
  const int f = (lane >> 1) & 7;
  int wo[4];
#pragma unroll
  for (int ks = 0; ks < 4; ++ks) wo[ks] = (wt * 64 + r32) * 128 + (((2 * ks + h) ^ f) << 4);
  const int blk = (lane >> 4) & 1, q = (lane & 15) >> 2, p = lane & 3;
  int xo0, xo1;
  if constexpr (ND == 2) {
    const int ch = wd * 8 + 2 * blk + (p >> 1);
    xo0 = 16384 + (8 * h + q) * 256 + ((ch ^ (q << 2)) << 4) + 8 * (p & 1);
    xo1 = xo0 ^ 64;
  } else {
    const int ch = wd * 4 + 2 * blk + (p >> 1);
    xo0 = 16384 + (8 * h + q) * 128 + ((ch ^ (((q >> 1) & 1) << 2)) << 4) + 8 * (p & 1);
    xo1 = xo0;
  }
  f32x16 acc[ND][2];
#pragma unroll
  for (int dd = 0; dd < ND; ++dd)
#pragma unroll
    for (int tt = 0; tt < 2; ++tt)
#pragma unroll
      for (int i = 0; i < 16; ++i) acc[dd][tt][i] = 0.f;
  Frags<ND> F0, F1, F2, F3;
  BAR();
  STAMP(1);
  loadFrags<ND>(F0, L + wo[0], L + xo0, L + xo1, 0);
  loadFrags<ND>(F1, L + wo[1], L + xo0, L + xo1, 1);
  SB();
  loadFrags<ND>(F2, L + wo[2], L + xo0, L + xo1, 2);
  mma<ND>(acc, F0);
  loadFrags<ND>(F3, L + wo[3], L + xo0, L + xo1, 3);
  mma<ND>(acc, F1);
  LBAR();
  SB();
#pragma unroll 1
  for (int s = 1; s < 16; ++s) {
    LAS unsigned char* B = L + (s & 3) * SLOT;
    loadFrags<ND>(F0, B + wo[0], B + xo0, B + xo1, 0);
    mma<ND>(acc, F2);
    loadFrags<ND>(F1, B + wo[1], B + xo0, B + xo1, 1);
    mma<ND>(acc, F3);
    loadFrags<ND>(F2, B + wo[2], B + xo0, B + xo1, 2);
    mma<ND>(acc, F0);
    loadFrags<ND>(F3, B + wo[3], B + xo0, B + xo1, 3);
    mma<ND>(acc, F1);
    if (s < 15) LBAR();
    SB();
  }
  STAMP(2);
#pragma unroll
  for (int dd = 0; dd < ND; ++dd)
#pragma unroll
    for (int tt = 0; tt < 2; ++tt) {
      acc[dd][tt] = mfma32(F2.x[dd], F2.w[tt], acc[dd][tt]);
      acc[dd][tt] = mfma32(F3.x[dd], F3.w[tt], acc[dd][tt]);
    }
#pragma unroll
  for (int dd = 0; dd < ND; ++dd)
#pragma unroll
    for (int tt = 0; tt < 2; ++tt) {
      const int kap = ND == 2 ? 4 * j + 2 * wd + dd : 12 + wd;
      const int tau = wt * 4 + tt * 2 + (r32 >> 4);
      const int base = ((((panel * 8 + tau) * 2 + part) * 2 + kh) * 14 + kap) * 1024 + (h * 16 + (lane & 15)) * 16;
#pragma unroll
      for (int u = 0; u < 2; ++u) {
        v4u o;
#pragma unroll
        for (int i = 0; i < 4; ++i) o[i] = pk2(acc[dd][tt][8 * u + 2 * i], acc[dd][tt][8 * u + 2 * i + 1]);
        const auto s0 = __builtin_amdgcn_permlane32_swap(o[0], o[2], false, false);
        const auto s1 = __builtin_amdgcn_permlane32_swap(o[1], o[3], false, false);
        __builtin_amdgcn_raw_buffer_store_b128(v4u{s0[0], s1[0], s0[1], s1[1]}, rs, base + u * 512, 0, AUX_SC1);
      }
    }
}

template <int ND>
DI void loader(LAS unsigned char* L, int lw, int lane, const u16* W, const u16* x, const u16* Wd, int panel, int kh, int j, int mi) {
  const __amdgpu_buffer_rsrc_t rw = __builtin_amdgcn_make_buffer_rsrc((void*)W, 0, SEQ * SEQ * 2, RSRC_W3);
  const __amdgpu_buffer_rsrc_t rx = __builtin_amdgcn_make_buffer_rsrc((void*)x, 0, SEQ * DH * 2, RSRC_W3);
  const __amdgpu_buffer_rsrc_t rd = __builtin_amdgcn_make_buffer_rsrc((void*)Wd, 0, DH * DH * 2, RSRC_W3);
  const int sz = (((lane & 7) ^ ((4 * (lw & 1) + (lane >> 4)) & 7)) << 4);
  const int vw = (lane >> 3) * 4096 + sz, vd = (lane >> 3) * 128 + sz;
  const int vx = ND == 2 ? (lane >> 4) * 896 + (((lane & 15) ^ ((lane >> 4) << 2)) << 4)
                         : (lane >> 3) * 896 + (((lane & 7) ^ (((lane >> 4) & 1) << 2)) << 4);
  const int sw0 = (panel * 128 + 8 * lw) * 4096 + kh * 2048;
  const int sx0 = (kh * 1024 + (ND == 2 ? 4 : 8) * lw) * 896 + j * 256;
  const int sd0 = j * 112 * 896 + (lw & 1) * 1024;
  int et = mi == 7 ? 0 : mi;
  const u64 otab = j == 0 ? 0xfedcba9876543210ull : j == 1 ? 0xfed9cba587614320ull : j == 2 ? 0xdfea9cb658721430ull : 0xedfba9c765832140ull;
  constexpr int a = 4 + 2 * ND, c = 7;
  auto A = [&](int n) {
    const int kst = ((int)(otab >> (4 * n)) + 2 * mi) & 15;
    LAS unsigned char* B = L + (n & 3) * SLOT + lw * 1024;
    const int sw = sw0 + kst * 128, sx = sx0 + kst * 64 * 896;
#pragma unroll
    for (int q = 0; q < 4; ++q) dma16(rw, B + q * 4096, vw, sw + q * 32 * 4096);
#pragma unroll
    for (int q = 0; q < 2 * ND; ++q) dma16(rx, B + 16384 + q * 4096, vx, sx + q * (ND == 2 ? 16 : 32) * 896);
  };
  auto Q = [&](int so) {
    LAS unsigned char* B = L + so + (lw >> 1) * 14336 + (lw & 1) * 1024;
    const int sd = sd0 + et * 14336;
#pragma unroll
    for (int i = 0; i < 7; ++i) dma16(rd, B + i * 2048, vd, sd + i * 2048);
    et = et == 6 ? 0 : et + 1;
  };
#pragma unroll 1
  for (int n = 0; n < 16; ++n) {
    if (n >= 2) vbar<a>();
    A(n);
  }
  vbar<a>(); Q(XSLOT);
  vbar<c>(); Q(0); Q(SLOT);
  vbar<c>(); Q(2 * SLOT); Q(3 * SLOT);
  BAR(); Q(0); Q(XSLOT);
}

__global__ __launch_bounds__(512, 2) void fnet(const u16* __restrict__ x, const u16* __restrict__ sre, const u16* __restrict__ sim,
                                               const u16* __restrict__ dre, const u16* __restrict__ dim, void* ws, u16* __restrict__ y) {
  __shared__ __attribute__((aligned(16))) unsigned char smem[LDS_BYTES];
  LAS unsigned char* L = (LAS unsigned char*)smem;
  const int lane = threadIdx.x & 63, w = __builtin_amdgcn_readfirstlane(threadIdx.x >> 6);
  const int b = blockIdx.x, xcd = b & 7, r = b >> 3;
  const int kh = xcd & 1, mi = r >> 2, j = r & 3;
  const int panel = (xcd >> 1) * 4 + (mi >> 1), part = mi & 1;
  const int mb = kh * 2 + part, nb = j;
  STAMP(0);
  if (w >= 4) {
    const int lw = w - 4;
    const u16* W = part ? sim : sre;
    const u16* Wd = (lw >> 1) ? dim : dre;
    if (j < 3) loader<2>(L, lw, lane, W, x, Wd, panel, kh, j, mi);
    else loader<1>(L, lw, lane, W, x, Wd, panel, kh, j, mi);
    WAITV0();
    STAMP(2);
    BAR();
    BAR();
  } else {
    const int cw = w;
    const __amdgpu_buffer_rsrc_t rs = __builtin_amdgcn_make_buffer_rsrc(ws, 0, WS_BYTES, RSRC_W3);
    const v2u ep = __builtin_amdgcn_raw_buffer_load_b64(rs, 0, EPOCH_OFF + panel * 256, AUX_SC1);
    if (cw == 0) {
      if (b < 196) {
        const unsigned char* p = (const unsigned char*)(b < 98 ? dre : dim) + (b < 98 ? b : b - 98) * 4096 + lane * 64;
        const unsigned tv = *(const volatile GAS unsigned*)p;
        asm volatile("" :: "v"(tv));
      }
    }
    if (cw == 1) {
      const int k = lane >> 3;
      const unsigned char* p = k < 2 ? (const unsigned char*)dre + nb * 100352 + (k & 1) * 100224
                             : k < 4 ? (const unsigned char*)dim + nb * 100352 + (k & 1) * 100224
                             : k < 6 ? (const unsigned char*)y + (size_t)(panel * 128 + mb * 32 + (k & 1) * 31) * 896
                                     : (const unsigned char*)ws + (size_t)panel * 458752 + (k & 1) * 458624;
      const unsigned tv = __hip_atomic_load((const GAS unsigned*)p, __ATOMIC_RELAXED, __HIP_MEMORY_SCOPE_AGENT);
      asm volatile("" :: "v"(tv));
    }
    const int pb = cw >> 1, ti = cw & 1;
    const int g = lane >> 4, e = lane & 15;
    const int krot = nb;
    const int erot = mi == 7 ? 0 : mi;
    int wa[14];
#pragma unroll
    for (int i = 0; i < 14; ++i) {
      int kap = i + krot; kap = kap >= 14 ? kap - 14 : kap;
      const int Ln = 7 * e + (kap >> 1), c = 4 * (kap & 1) + g;
      wa[i] = pb * 14336 + Ln * 128 + ((c ^ ((Ln >> 1) & 7)) << 4);
      asm volatile("" : "+v"(wa[i]));
    }
    const u64 tag = ((u64)(unsigned)__builtin_amdgcn_readfirstlane(ep.y) << 32 | (unsigned)__builtin_amdgcn_readfirstlane(ep.x)) + 1;
    const __amdgpu_buffer_rsrc_t ry = __builtin_amdgcn_make_buffer_rsrc((void*)y, 0, SEQ * DH * 2, RSRC_W3);
    asm volatile("" :: "s"(y), "s"(tag));
    if (j < 3) phaseA_compute<2>(L, cw, lane, ws, rs, panel, part, kh, j);
    else phaseA_compute<1>(L, cw, lane, ws, rs, panel, part, kh, j);
    LBAR();
    STAMP(3);
    bf16x8 wr[2][14];
#pragma unroll
    for (int i = 0; i < 14; ++i) { wr[0][i] = ldW2(L, XSLOT, wa[i]); wr[1][i] = ldW2(L, 0, wa[i]); }
    WAITV0();
    STAMP(5);
    {
      const int wt = cw & 1, wd = cw >> 1;
      const int cmb = 2 * wt + ((lane >> 2) & 1), cnb = lane & 3;
      const int cons = (panel * 4 + cmb) * 4 + cnb;
      const int ent = (part * 2 + kh) * 8 + 2 * j + wd;
      if (lane < 8) __builtin_amdgcn_raw_buffer_store_b64(v2u{(unsigned)tag, (unsigned)(tag >> 32)}, rs, FLAG_OFF + cons * 256 + ent * 8, 0, AUX_SC1);
    }
    LBAR();
    bf16x8 wf[2][5];
    constexpr int so[5] = {SLOT, 2 * SLOT, 3 * SLOT, 0, XSLOT};
    BAR();
#pragma unroll
    for (int q = 0; q < 5; ++q) wf[0][q] = ldW2(L, so[q], wa[0]);
    {
      const int cons = (panel * 4 + mb) * 4 + nb;
      unsigned spins = 0;
      for (;;) {
        const v2u v = __builtin_amdgcn_raw_buffer_load_b64(rs, (lane & 15) * 8, FLAG_OFF + cons * 256 + pb * 128, AUX_SC1);
        const bool ok = v.x == (unsigned)tag && v.y == (unsigned)(tag >> 32);
        if (__all(ok) || ++spins > (1u << 16)) break;
        __builtin_amdgcn_s_sleep(1);
      }
      __builtin_amdgcn_fence(__ATOMIC_ACQUIRE, "wavefront");
    }
    STAMP(6);
    constexpr int PD = 4;
    v4u pq[14][2];
    const int pqb = (((panel * 8 + mb * 2 + ti) * 2 + pb) * 2) * 14 * 1024;
    auto ldpq = [&](int i) {
      int kap = i + krot; kap = kap >= 14 ? kap - 14 : kap;
#pragma unroll
      for (int h2 = 0; h2 < 2; ++h2) pq[i][h2] = __builtin_amdgcn_raw_buffer_load_b128(rs, lane * 16, pqb + (h2 * 14 + kap) * 1024, AUX_SC1);
    };
#pragma unroll
    for (int i = 0; i < PD; ++i) ldpq(i);
    SB();
    f32x4 acc[7];
#pragma unroll
    for (int q = 0; q < 7; ++q) acc[q] = f32x4{0, 0, 0, 0};
#pragma unroll
    for (int i = 0; i < 14; ++i) {
      if (i + PD < 14) ldpq(i + PD);
      if (i + 1 < 14) {
#pragma unroll
        for (int q = 0; q < 5; ++q) wf[(i + 1) & 1][q] = ldW2(L, so[q], wa[i + 1]);
      }
#pragma unroll
      for (int h2 = 0; h2 < 2; ++h2)
#pragma unroll
        for (int q = 0; q < 7; ++q) acc[q] = mfma16(q < 2 ? wr[q][i] : wf[i & 1][q - 2], __builtin_bit_cast(bf16x8, pq[i][h2]), acc[q]);
#pragma unroll
      for (int q = 0; q < 5; ++q) { SGB(0x8, 1); SGB(0x100, 1); }
      SGB(0x8, 1); SGB(0x4, 2); SGB(0x8, 1); SGB(0x4, 2); SGB(0x8, 1); SGB(0x4, 2);
      SGB(0x8, 1); SGB(0x20, 1); SGB(0x8, 2); SGB(0x20, 1); SGB(0x8, 3);
      SB();
    }
    STAMP(7);
    LAS unsigned char* X = L + 28672 + lane * 16;
#pragma unroll
    for (int q = 0; q < 7; ++q) {
      const int t = ti * 7 + q;
      if ((q < 4) == (pb == 1)) *(LAS f32x4*)(X + (t >> 2) * SLOT + (t & 3) * 1024) = acc[q];
    }
    LBAR();
    if (threadIdx.x == 0 && mb == 0 && nb == 0)
      __builtin_amdgcn_raw_buffer_store_b64(v2u{(unsigned)tag, (unsigned)(tag >> 32)}, rs, EPOCH_OFF + panel * 256, 0, AUX_SC1);
    const int yrow = ((panel * 128 + mb * 32 + ti * 16 + e) * DH + nb * 112) * 2;
    auto fin = [&](int q) { const int t = ti * 7 + q; const f32x4 o = *(LAS f32x4*)(X + (t >> 2) * SLOT + (t & 3) * 1024); return q < 4 ? acc[q] - o : o - acc[q]; };
    auto st2 = [&](int qa, int qb) {
      const f32x4 a = fin(qa), c = fin(qb);
      const auto s0 = __builtin_amdgcn_permlane16_swap(pk2(a[0], a[1]), pk2(c[0], c[1]), false, false);
      const auto s1 = __builtin_amdgcn_permlane16_swap(pk2(a[2], a[3]), pk2(c[2], c[3]), false, false);
      int et = ((g & 1) ? qb : qa) + erot; et = et >= 7 ? et - 7 : et;
      __builtin_amdgcn_raw_buffer_store_b128(v4u{s0[0], s1[0], s0[1], s1[1]}, ry, yrow + 32 * et + 16 * (g >> 1), 0, 0);
    };
    if (pb == 0) { st2(0, 1); st2(2, 3); }
    else {
      st2(4, 5);
      const f32x4 a = fin(6);
      int et = 6 + erot; et = et >= 7 ? et - 7 : et;
      __builtin_amdgcn_raw_buffer_store_b64(v2u{pk2(a[0], a[1]), pk2(a[2], a[3])}, ry, yrow + 32 * et + 8 * g, 0, 0);
    }
  }
  STAMP(8);
}

#define RT(b,w,i) (sp[((size_t)(b) * 8 + (w)) * 32 + 2 * (i)])
#define CY(b,w,i) (sp[((size_t)(b) * 8 + (w)) * 32 + 2 * (i) + 1])
__device__ unsigned long long minrt(const volatile unsigned long long* sp, int w, int i) { unsigned long long m = ~0ull; for (int b = 0; b < 256; ++b) { unsigned long long t = RT(b,w,i); m = t < m ? t : m; } return m; }
__device__ unsigned long long maxrt(const volatile unsigned long long* sp, int w, int i) { unsigned long long m = 0; for (int b = 0; b < 256; ++b) { unsigned long long t = RT(b,w,i); m = t > m ? t : m; } return m; }
__device__ unsigned long long avgd(const volatile unsigned long long* sp, int w, int i, int j) { unsigned long long m = 0; for (int b = 0; b < 256; ++b) { m += RT(b,w,j) - RT(b,w,i); } return m / 256; }
__device__ unsigned long long avgc(const volatile unsigned long long* sp, int w, int i, int j) { unsigned long long m = 0; for (int b = 0; b < 256; ++b) { m += CY(b,w,j) - CY(b,w,i); } return m / 256; }
#define MINRT(w,i) minrt(sp,w,i)
#define MAXRT(w,i) maxrt(sp,w,i)
#define AVGD(w,i,j) avgd(sp,w,i,j)
#define AVGC(w,i,j) avgc(sp,w,i,j)
__global__ void kPre(void* ws) { ((volatile unsigned long long*)((char*)ws + STAMP_OFF + (16 << 20)))[0] = __builtin_amdgcn_s_memrealtime(); }
#define PRET (((const volatile unsigned long long*)((char*)ws + STAMP_OFF + (16 << 20)))[0])
__global__ void kProbe(void* ws) {
  const volatile unsigned long long* sp = (const volatile unsigned long long*)((char*)ws + STAMP_OFF);
  unsigned long long t0 = __builtin_amdgcn_s_memrealtime();
  unsigned long long val = (0);
  if (val > 5000) val = 5000;
  unsigned long long tend = t0 + 15000 + 10 * val;
  while (__builtin_amdgcn_s_memrealtime() < tend) __builtin_amdgcn_s_sleep(1);
}
extern "C" void kernel_launch(void* const* d_in, const int* in_sizes, int n_in, void* d_out, int out_size, void* d_ws, size_t ws_size,
                              hipStream_t stream) {
  fnet<<<256, 512, 0, stream>>>((const u16*)d_in[0], (const u16*)d_in[1], (const u16*)d_in[2], (const u16*)d_in[3], (const u16*)d_in[4], d_ws, (u16*)d_out);
  kProbe<<<1, 1, 0, stream>>>(d_ws);
}
```

```cpp
#include <hip/hip_runtime.h>
typedef unsigned short u16;
typedef unsigned long long u64;
typedef short s16x4 __attribute__((ext_vector_type(4)));
typedef short bf16x8 __attribute__((ext_vector_type(8)));
typedef float f32x4 __attribute__((ext_vector_type(4)));
typedef float f32x16 __attribute__((ext_vector_type(16)));
typedef unsigned v4u __attribute__((ext_vector_type(4)));
typedef unsigned v2u __attribute__((ext_vector_type(2)));
typedef __bf16 bf2 __attribute__((ext_vector_type(2)));
typedef float f32x2 __attribute__((ext_vector_type(2)));
#define GAS __attribute__((address_space(1)))
#define LAS __attribute__((address_space(3)))
#define DI __device__ __forceinline__

constexpr int SEQ = 2048, DH = 448;
constexpr int SLOT = 32768;
constexpr int XSLOT = 4 * SLOT;
constexpr int LDS_BYTES = 163840;
constexpr int PQ_BYTES = 16 * 8 * 2 * 2 * 14 * 1024;
constexpr int FLAG_OFF = PQ_BYTES;
constexpr int EPOCH_OFF = FLAG_OFF + 1024 * 128;
constexpr int WS_BYTES = EPOCH_OFF + 16 * 256;
constexpr int RSRC_W3 = 0x00020000;
constexpr int AUX_SC1 = 16;


DI unsigned pk2(float a, float b) { bf2 r = __builtin_convertvector(f32x2{a, b}, bf2); return __builtin_bit_cast(unsigned, r); }
DI void dma16(__amdgpu_buffer_rsrc_t rs, LAS unsigned char* dst, int voff, int soff) {
  __builtin_amdgcn_raw_ptr_buffer_load_lds(rs, (LAS void*)dst, 16, voff, soff, 0, 0);
}
DI s16x4 trd(LAS unsigned char* p) { return __builtin_amdgcn_ds_read_tr16_b64_v4i16((LAS s16x4*)p); }
DI bf16x8 cat(s16x4 a, s16x4 b) { return __builtin_shufflevector(a, b, 0, 1, 2, 3, 4, 5, 6, 7); }
DI f32x4 mfma16(bf16x8 a, bf16x8 b, f32x4 c) { return __builtin_amdgcn_mfma_f32_16x16x32_bf16(a, b, c, 0, 0, 0); }
DI f32x16 mfma32(bf16x8 a, bf16x8 b, f32x16 c) { return __builtin_amdgcn_mfma_f32_32x32x16_bf16(a, b, c, 0, 0, 0); }
template <int N> DI void vbar() { asm volatile("s_waitcnt vmcnt(%0)\n\ts_barrier" :: "i"(N) : "memory"); }
#define WAITV0() asm volatile("s_waitcnt vmcnt(0)" ::: "memory")
#define LBAR() asm volatile("s_waitcnt lgkmcnt(0)\n\ts_barrier" ::: "memory")
#define BAR() asm volatile("s_barrier" ::: "memory")
#define SB() __builtin_amdgcn_sched_barrier(0)
#define SGB(m, n) __builtin_amdgcn_sched_group_barrier(m, n, 0)
DI bf16x8 ldW2(LAS unsigned char* L, int so, int a) { return *(LAS bf16x8*)(L + so + a); }

template <int ND> struct Frags { bf16x8 w[2]; bf16x8 x[ND]; };
template <int ND>
DI void loadFrags(Frags<ND>& f, LAS unsigned char* wA, LAS unsigned char* x0, LAS unsigned char* x1, int ks) {
  constexpr int RP = ND == 2 ? 256 : 128;
  f.x[0] = cat(trd(x0 + (16 * ks) * RP), trd(x0 + (16 * ks + 4) * RP));
  f.w[0] = *(LAS bf16x8*)(wA);
  f.w[1] = *(LAS bf16x8*)(wA + 4096);
  if constexpr (ND == 2) f.x[1] = cat(trd(x1 + (16 * ks) * RP), trd(x1 + (16 * ks + 4) * RP));
}
template <int ND> DI void mma(f32x16 (&acc)[ND][2], const Frags<ND>& f) {
#pragma unroll
  for (int dd = 0; dd < ND; ++dd)
#pragma unroll
    for (int tt = 0; tt < 2; ++tt) acc[dd][tt] = mfma32(f.x[dd], f.w[tt], acc[dd][tt]);
  if constexpr (ND == 2) { SGB(0x8, 1); SGB(0x100, 3); SGB(0x8, 1); SGB(0x100, 3); SGB(0x8, 2); }
  else { SGB(0x8, 1); SGB(0x100, 4); SGB(0x8, 1); }
  SB();
}

template <int ND>
DI void phaseA_compute(LAS unsigned char* L, int cw, int lane, void* ws, __amdgpu_buffer_rsrc_t rs, int panel, int part, int kh, int j) {
  const int wt = cw & 1, wd = cw >> 1, h = lane >> 5, r32 = lane & 31;
  const int f = (lane >> 1) & 7;
  int wo[4];
#pragma unroll
  for (int ks = 0; ks < 4; ++ks) wo[ks] = (wt * 64 + r32) * 128 + (((2 * ks + h) ^ f) << 4);
  const int blk = (lane >> 4) & 1, q = (lane & 15) >> 2, p = lane & 3;
  int xo0, xo1;
  if constexpr (ND == 2) {
    const int ch = wd * 8 + 2 * blk + (p >> 1);
    xo0 = 16384 + (8 * h + q) * 256 + ((ch ^ (q << 2)) << 4) + 8 * (p & 1);
    xo1 = xo0 ^ 64;
  } else {
    const int ch = wd * 4 + 2 * blk + (p >> 1);
    xo0 = 16384 + (8 * h + q) * 128 + ((ch ^ (((q >> 1) & 1) << 2)) << 4) + 8 * (p & 1);
    xo1 = xo0;
  }
  f32x16 acc[ND][2];
#pragma unroll
  for (int dd = 0; dd < ND; ++dd)
#pragma unroll
    for (int tt = 0; tt < 2; ++tt)
#pragma unroll
      for (int i = 0; i < 16; ++i) acc[dd][tt][i] = 0.f;
  Frags<ND> F0, F1, F2, F3;
  BAR();
  loadFrags<ND>(F0, L + wo[0], L + xo0, L + xo1, 0);
  loadFrags<ND>(F1, L + wo[1], L + xo0, L + xo1, 1);
  SB();
  loadFrags<ND>(F2, L + wo[2], L + xo0, L + xo1, 2);
  mma<ND>(acc, F0);
  loadFrags<ND>(F3, L + wo[3], L + xo0, L + xo1, 3);
  mma<ND>(acc, F1);
  LBAR();
  SB();
#pragma unroll 1
  for (int s = 1; s < 16; ++s) {
    LAS unsigned char* B = L + (s & 3) * SLOT;
    loadFrags<ND>(F0, B + wo[0], B + xo0, B + xo1, 0);
    mma<ND>(acc, F2);
    loadFrags<ND>(F1, B + wo[1], B + xo0, B + xo1, 1);
    mma<ND>(acc, F3);
    loadFrags<ND>(F2, B + wo[2], B + xo0, B + xo1, 2);
    mma<ND>(acc, F0);
    loadFrags<ND>(F3, B + wo[3], B + xo0, B + xo1, 3);
    mma<ND>(acc, F1);
    if (s < 15) LBAR();
    SB();
  }
#pragma unroll
  for (int dd = 0; dd < ND; ++dd)
#pragma unroll
    for (int tt = 0; tt < 2; ++tt) {
      acc[dd][tt] = mfma32(F2.x[dd], F2.w[tt], acc[dd][tt]);
      acc[dd][tt] = mfma32(F3.x[dd], F3.w[tt], acc[dd][tt]);
    }
#pragma unroll
  for (int dd = 0; dd < ND; ++dd)
#pragma unroll
    for (int tt = 0; tt < 2; ++tt) {
      const int kap = ND == 2 ? 4 * j + 2 * wd + dd : 12 + wd;
      const int tau = wt * 4 + tt * 2 + (r32 >> 4);
      const int base = ((((panel * 8 + tau) * 2 + part) * 2 + kh) * 14 + kap) * 1024 + (h * 16 + (lane & 15)) * 16;
#pragma unroll
      for (int u = 0; u < 2; ++u) {
        v4u o;
#pragma unroll
        for (int i = 0; i < 4; ++i) o[i] = pk2(acc[dd][tt][8 * u + 2 * i], acc[dd][tt][8 * u + 2 * i + 1]);
        const auto s0 = __builtin_amdgcn_permlane32_swap(o[0], o[2], false, false);
        const auto s1 = __builtin_amdgcn_permlane32_swap(o[1], o[3], false, false);
        __builtin_amdgcn_raw_buffer_store_b128(v4u{s0[0], s1[0], s0[1], s1[1]}, rs, base + u * 512, 0, AUX_SC1);
      }
    }
}

template <int ND>
DI void loader(LAS unsigned char* L, int lw, int lane, const u16* W, const u16* x, const u16* Wd, int panel, int kh, int j, int mi) {
  const __amdgpu_buffer_rsrc_t rw = __builtin_amdgcn_make_buffer_rsrc((void*)W, 0, SEQ * SEQ * 2, RSRC_W3);
  const __amdgpu_buffer_rsrc_t rx = __builtin_amdgcn_make_buffer_rsrc((void*)x, 0, SEQ * DH * 2, RSRC_W3);
  const __amdgpu_buffer_rsrc_t rd = __builtin_amdgcn_make_buffer_rsrc((void*)Wd, 0, DH * DH * 2, RSRC_W3);
  const int sz = (((lane & 7) ^ ((4 * (lw & 1) + (lane >> 4)) & 7)) << 4);
  const int vw = (lane >> 3) * 4096 + sz, vd = (lane >> 3) * 128 + sz;
  const int vx = ND == 2 ? (lane >> 4) * 896 + (((lane & 15) ^ ((lane >> 4) << 2)) << 4)
                         : (lane >> 3) * 896 + (((lane & 7) ^ (((lane >> 4) & 1) << 2)) << 4);
  const int sw0 = (panel * 128 + 8 * lw) * 4096 + kh * 2048;
  const int sx0 = (kh * 1024 + (ND == 2 ? 4 : 8) * lw) * 896 + j * 256;
  const int sd0 = j * 112 * 896 + (lw & 1) * 1024;
  int et = mi == 7 ? 0 : mi;
  const u64 otab = j == 0 ? 0xfedcba9876543210ull : j == 1 ? 0xfed9cba587614320ull : j == 2 ? 0xdfea9cb658721430ull : 0xedfba9c765832140ull;
  constexpr int a = 4 + 2 * ND, c = 7;
  auto A = [&](int n) {
    const int kst = ((int)(otab >> (4 * n)) + 2 * mi) & 15;
    LAS unsigned char* B = L + (n & 3) * SLOT + lw * 1024;
    const int sw = sw0 + kst * 128, sx = sx0 + kst * 64 * 896;
#pragma unroll
    for (int q = 0; q < 4; ++q) dma16(rw, B + q * 4096, vw, sw + q * 32 * 4096);
#pragma unroll
    for (int q = 0; q < 2 * ND; ++q) dma16(rx, B + 16384 + q * 4096, vx, sx + q * (ND == 2 ? 16 : 32) * 896);
  };
  auto Q = [&](int so) {
    LAS unsigned char* B = L + so + (lw >> 1) * 14336 + (lw & 1) * 1024;
    const int sd = sd0 + et * 14336;
#pragma unroll
    for (int i = 0; i < 7; ++i) dma16(rd, B + i * 2048, vd, sd + i * 2048);
    et = et == 6 ? 0 : et + 1;
  };
#pragma unroll 1
  for (int n = 0; n < 16; ++n) {
    if (n >= 2) vbar<a>();
    A(n);
  }
  vbar<a>(); Q(XSLOT);
  vbar<c>(); Q(0); Q(SLOT);
  vbar<c>(); Q(2 * SLOT); Q(3 * SLOT);
  BAR(); Q(0); Q(XSLOT);
}

__global__ __launch_bounds__(512, 2) void fnet(const u16* __restrict__ x, const u16* __restrict__ sre, const u16* __restrict__ sim,
                                               const u16* __restrict__ dre, const u16* __restrict__ dim, void* ws, u16* __restrict__ y) {
  __shared__ __attribute__((aligned(16))) unsigned char smem[LDS_BYTES];
  LAS unsigned char* L = (LAS unsigned char*)smem;
  const int lane = threadIdx.x & 63, w = __builtin_amdgcn_readfirstlane(threadIdx.x >> 6);
  const int b = blockIdx.x, xcd = b & 7, r = b >> 3;
  const int kh = xcd & 1, mi = r >> 2, j = r & 3;
  const int panel = (xcd >> 1) * 4 + (mi >> 1), part = mi & 1;
  const int mb = kh * 2 + part, nb = j;
  if (w >= 4) {
    const int lw = w - 4;
    const u16* W = part ? sim : sre;
    const u16* Wd = (lw >> 1) ? dim : dre;
    if (j < 3) loader<2>(L, lw, lane, W, x, Wd, panel, kh, j, mi);
    else loader<1>(L, lw, lane, W, x, Wd, panel, kh, j, mi);
    WAITV0();
    BAR();
    BAR();
  } else {
    const int cw = w;
    const __amdgpu_buffer_rsrc_t rs = __builtin_amdgcn_make_buffer_rsrc(ws, 0, WS_BYTES, RSRC_W3);
    const v2u ep = __builtin_amdgcn_raw_buffer_load_b64(rs, 0, EPOCH_OFF + panel * 256, AUX_SC1);
    if (cw == 0) {
      if (b < 196) {
        const unsigned char* p = (const unsigned char*)(b < 98 ? dre : dim) + (b < 98 ? b : b - 98) * 4096 + lane * 64;
        const unsigned tv = *(const volatile GAS unsigned*)p;
        asm volatile("" :: "v"(tv));
      }
    }
    if (cw == 1) {
      const int k = lane >> 3;
      const unsigned char* p = k < 2 ? (const unsigned char*)dre + nb * 100352 + (k & 1) * 100224
                             : k < 4 ? (const unsigned char*)dim + nb * 100352 + (k & 1) * 100224
                             : k < 6 ? (const unsigned char*)y + (size_t)(panel * 128 + mb * 32 + (k & 1) * 31) * 896
                                     : (const unsigned char*)ws + (size_t)panel * 458752 + (k & 1) * 458624;
      const unsigned tv = __hip_atomic_load((const GAS unsigned*)p, __ATOMIC_RELAXED, __HIP_MEMORY_SCOPE_AGENT);
      asm volatile("" :: "v"(tv));
    }
    const int pb = cw >> 1, ti = cw & 1;
    const int g = lane >> 4, e = lane & 15;
    const int krot = nb;
    const int erot = mi == 7 ? 0 : mi;
    int wa[14];
#pragma unroll
    for (int i = 0; i < 14; ++i) {
      int kap = i + krot; kap = kap >= 14 ? kap - 14 : kap;
      const int Ln = 7 * e + (kap >> 1), c = 4 * (kap & 1) + g;
      wa[i] = pb * 14336 + Ln * 128 + ((c ^ ((Ln >> 1) & 7)) << 4);
      asm volatile("" : "+v"(wa[i]));
    }
    const u64 tag = ((u64)(unsigned)__builtin_amdgcn_readfirstlane(ep.y) << 32 | (unsigned)__builtin_amdgcn_readfirstlane(ep.x)) + 1;
    const __amdgpu_buffer_rsrc_t ry = __builtin_amdgcn_make_buffer_rsrc((void*)y, 0, SEQ * DH * 2, RSRC_W3);
    asm volatile("" :: "s"(y), "s"(tag));
    if (j < 3) phaseA_compute<2>(L, cw, lane, ws, rs, panel, part, kh, j);
    else phaseA_compute<1>(L, cw, lane, ws, rs, panel, part, kh, j);
    LBAR();
    bf16x8 wr[2][14];
#pragma unroll
    for (int i = 0; i < 14; ++i) { wr[0][i] = ldW2(L, XSLOT, wa[i]); wr[1][i] = ldW2(L, 0, wa[i]); }
    WAITV0();
    {
      const int wt = cw & 1, wd = cw >> 1;
      const int cmb = 2 * wt + ((lane >> 2) & 1), cnb = lane & 3;
      const int cons = (panel * 4 + cmb) * 4 + cnb;
      const int ent = (part * 2 + kh) * 8 + 2 * j + wd;
      if (lane < 8) __builtin_amdgcn_raw_buffer_store_b64(v2u{(unsigned)tag, (unsigned)(tag >> 32)}, rs, FLAG_OFF + cons * 256 + ent * 8, 0, AUX_SC1);
    }
    LBAR();
    bf16x8 wf[2][5];
    constexpr int so[5] = {SLOT, 2 * SLOT, 3 * SLOT, 0, XSLOT};
    BAR();
#pragma unroll
    for (int q = 0; q < 5; ++q) wf[0][q] = ldW2(L, so[q], wa[0]);
    {
      const int cons = (panel * 4 + mb) * 4 + nb;
      unsigned spins = 0;
      for (;;) {
        const v2u v = __builtin_amdgcn_raw_buffer_load_b64(rs, (lane & 15) * 8, FLAG_OFF + cons * 256 + pb * 128, AUX_SC1);
        const bool ok = v.x == (unsigned)tag && v.y == (unsigned)(tag >> 32);
        if (__all(ok) || ++spins > (1u << 16)) break;
        __builtin_amdgcn_s_sleep(1);
      }
      __builtin_amdgcn_fence(__ATOMIC_ACQUIRE, "wavefront");
    }
    constexpr int PD = 4;
    v4u pq[14][2];
    const int pqb = (((panel * 8 + mb * 2 + ti) * 2 + pb) * 2) * 14 * 1024;
    auto ldpq = [&](int i) {
      int kap = i + krot; kap = kap >= 14 ? kap - 14 : kap;
#pragma unroll
      for (int h2 = 0; h2 < 2; ++h2) pq[i][h2] = __builtin_amdgcn_raw_buffer_load_b128(rs, lane * 16, pqb + (h2 * 14 + kap) * 1024, AUX_SC1);
    };
#pragma unroll
    for (int i = 0; i < PD; ++i) ldpq(i);
    SB();
    f32x4 acc[7];
#pragma unroll
    for (int q = 0; q < 7; ++q) acc[q] = f32x4{0, 0, 0, 0};
#pragma unroll
    for (int i = 0; i < 14; ++i) {
      if (i + PD < 14) ldpq(i + PD);
      if (i + 1 < 14) {
#pragma unroll
        for (int q = 0; q < 5; ++q) wf[(i + 1) & 1][q] = ldW2(L, so[q], wa[i + 1]);
      }
#pragma unroll
      for (int h2 = 0; h2 < 2; ++h2)
#pragma unroll
        for (int q = 0; q < 7; ++q) acc[q] = mfma16(q < 2 ? wr[q][i] : wf[i & 1][q - 2], __builtin_bit_cast(bf16x8, pq[i][h2]), acc[q]);
#pragma unroll
      for (int q = 0; q < 5; ++q) { SGB(0x8, 1); SGB(0x100, 1); }
      SGB(0x8, 1); SGB(0x4, 2); SGB(0x8, 1); SGB(0x4, 2); SGB(0x8, 1); SGB(0x4, 2);
      SGB(0x8, 1); SGB(0x20, 1); SGB(0x8, 2); SGB(0x20, 1); SGB(0x8, 3);
      SB();
    }
    LAS unsigned char* X = L + 28672 + lane * 16;
#pragma unroll
    for (int q = 0; q < 7; ++q) {
      const int t = ti * 7 + q;
      if ((q < 4) == (pb == 1)) *(LAS f32x4*)(X + (t >> 2) * SLOT + (t & 3) * 1024) = acc[q];
    }
    LBAR();
    if (threadIdx.x == 0 && mb == 0 && nb == 0)
      __builtin_amdgcn_raw_buffer_store_b64(v2u{(unsigned)tag, (unsigned)(tag >> 32)}, rs, EPOCH_OFF + panel * 256, 0, AUX_SC1);
    const int yrow = ((panel * 128 + mb * 32 + ti * 16 + e) * DH + nb * 112) * 2;
    auto fin = [&](int q) { const int t = ti * 7 + q; const f32x4 o = *(LAS f32x4*)(X + (t >> 2) * SLOT + (t & 3) * 1024); return q < 4 ? acc[q] - o : o - acc[q]; };
    auto st2 = [&](int qa, int qb) {
      const f32x4 a = fin(qa), c = fin(qb);
      const auto s0 = __builtin_amdgcn_permlane16_swap(pk2(a[0], a[1]), pk2(c[0], c[1]), false, false);
      const auto s1 = __builtin_amdgcn_permlane16_swap(pk2(a[2], a[3]), pk2(c[2], c[3]), false, false);
      int et = ((g & 1) ? qb : qa) + erot; et = et >= 7 ? et - 7 : et;
      __builtin_amdgcn_raw_buffer_store_b128(v4u{s0[0], s1[0], s0[1], s1[1]}, ry, yrow + 32 * et + 16 * (g >> 1), 0, 0);
    };
    if (pb == 0) { st2(0, 1); st2(2, 3); }
    else {
      st2(4, 5);
      const f32x4 a = fin(6);
      int et = 6 + erot; et = et >= 7 ? et - 7 : et;
      __builtin_amdgcn_raw_buffer_store_b64(v2u{pk2(a[0], a[1]), pk2(a[2], a[3])}, ry, yrow + 32 * et + 8 * g, 0, 0);
    }
  }
}

extern "C" void kernel_launch(void* const* d_in, const int* in_sizes, int n_in, void* d_out, int out_size, void* d_ws, size_t ws_size,
                              hipStream_t stream) {
  fnet<<<256, 512, 0, stream>>>((const u16*)d_in[0], (const u16*)d_in[1], (const u16*)d_in[2], (const u16*)d_in[3], (const u16*)d_in[4], d_ws, (u16*)d_out);
}
```

```cpp
#include <hip/hip_runtime.h>
#include <type_traits>
typedef unsigned short u16;
typedef unsigned long long u64;
typedef short s16x4 __attribute__((ext_vector_type(4)));
typedef short bf16x8 __attribute__((ext_vector_type(8)));
typedef float f32x4 __attribute__((ext_vector_type(4)));
typedef unsigned v4u __attribute__((ext_vector_type(4)));
typedef unsigned v2u __attribute__((ext_vector_type(2)));
typedef __bf16 bf2 __attribute__((ext_vector_type(2)));
typedef float f32x2 __attribute__((ext_vector_type(2)));
#define LAS __attribute__((address_space(3)))
#define DI __device__ __forceinline__

constexpr int SEQ = 2048, DH = 448;
constexpr int SLOT = 32768;
constexpr int XSLOT = 4 * SLOT;
constexpr int SPARE = XSLOT + 28672;
constexpr int LDS_BYTES = 163840;
constexpr int PQ_BYTES = 16 * 8 * 2 * 2 * 14 * 1024;
constexpr int FLAG_OFF = PQ_BYTES;
constexpr int EPOCH_OFF = FLAG_OFF + 131072 + 128;
constexpr int WS_BYTES = EPOCH_OFF + 16 * 256;
constexpr int RSRC_W3 = 0x00020000;
constexpr int AUX_SC1 = 16;
constexpr int OOB = 0x40000000;
constexpr int KROT = 6;


DI unsigned pk2(float a, float b) { bf2 r = __builtin_convertvector(f32x2{a, b}, bf2); return __builtin_bit_cast(unsigned, r); }
template <int IMM> DI void dma16(__amdgpu_buffer_rsrc_t rs, LAS unsigned char* dst, int voff, int soff) {
  __builtin_amdgcn_raw_ptr_buffer_load_lds(rs, (LAS void*)dst, 16, voff, soff, IMM, 0);
}
DI void dma4(__amdgpu_buffer_rsrc_t rs, LAS unsigned char* dst, int voff) {
  __builtin_amdgcn_raw_ptr_buffer_load_lds(rs, (LAS void*)dst, 4, voff, 0, 0, AUX_SC1);
}
DI s16x4 trd(LAS unsigned char* p) { return __builtin_amdgcn_ds_read_tr16_b64_v4i16((LAS s16x4*)p); }
DI bf16x8 cat(s16x4 a, s16x4 b) { return __builtin_shufflevector(a, b, 0, 1, 2, 3, 4, 5, 6, 7); }
DI f32x4 mfma16(bf16x8 a, bf16x8 b, f32x4 c) { return __builtin_amdgcn_mfma_f32_16x16x32_bf16(a, b, c, 0, 0, 0); }
template <int N> DI void vbar() { asm volatile("s_waitcnt vmcnt(%0)\n\ts_barrier" :: "i"(N) : "memory"); }
#define WAITV0() asm volatile("s_waitcnt vmcnt(0)" ::: "memory")
#define LBAR() asm volatile("s_waitcnt lgkmcnt(0)\n\ts_barrier" ::: "memory")
#define BAR() asm volatile("s_barrier" ::: "memory")
#define SB() __builtin_amdgcn_sched_barrier(0)
#define SGB(m, n) __builtin_amdgcn_sched_group_barrier(m, n, 0)
template <int I, int N, class F> DI void sfor(F&& f) {
  if constexpr (I < N) { f(std::integral_constant<int, I>{}); sfor<I + 1, N>(f); }
}
#define PAD(k, next) asm volatile(".Lfnet_pad" #k ":\n\ts_cbranch_execz .Lfnet_" #next)
#define PADA(k, next) asm volatile("s_branch .Lfnet_pad" #k "\n\t.p2align 12\n.Lfnet_pad" #k ":\n\ts_cbranch_execz .Lfnet_" #next)
#define HOP(i, a, b, c) asm volatile("s_mov_b64 exec, 0\n\ts_cmp_eq_u32 %0, 1\n\ts_cbranch_scc1 .Lfnet_" #a "\n\ts_cmp_eq_u32 %0, 2\n\ts_cbranch_scc1 .Lfnet_" #b \
   "\n\ts_cmp_eq_u32 %0, 3\n\ts_cbranch_scc1 .Lfnet_" #c "\n.Lfnet_ret:\n\ts_mov_b64 exec, -1" :: "s"(i) : "scc")

template <int ND> struct Frags { bf16x8 w[4]; bf16x8 x[2 * ND]; };
template <int ND>
DI void loadFrags(Frags<ND>& f, LAS unsigned char* B, int wo, const int (&xo)[2 * ND], int ks) {
  f.x[0] = cat(trd(B + xo[0] + (32 * ks) * 256), trd(B + xo[0] + (32 * ks + 4) * 256));
#pragma unroll
  for (int tt = 0; tt < 4; ++tt) f.w[tt] = *(LAS bf16x8*)(B + wo + tt * 2048);
#pragma unroll
  for (int dd = 1; dd < 2 * ND; ++dd) f.x[dd] = cat(trd(B + xo[dd] + (32 * ks) * 256), trd(B + xo[dd] + (32 * ks + 4) * 256));
}

template <int ND>
DI void phaseA_compute(LAS unsigned char* L, int cw, int lane, __amdgpu_buffer_rsrc_t rs, int panel, int part, int kh, int j) {
  if constexpr (ND == 1) { PAD(0, pad3); SB(); }
  constexpr int NX = 2 * ND;
  const int wt = cw & 1, wd = cw >> 1, g = lane >> 4, r16 = lane & 15;
  const int f = (lane >> 1) & 7;
  const int wo0 = (wt * 64 + r16) * 128 + ((g ^ f) << 4), wo1 = wo0 ^ 64;
  const int q = r16 >> 2, p = lane & 3;
  int xo[NX];
#pragma unroll
  for (int dd = 0; dd < NX; ++dd) { const int u = wd * NX + dd, hh = q | ((g & 1) << 2); xo[dd] = 16384 + (8 * g + q) * 256 + ((u ^ hh) << 5) + 8 * p; }
  int sa[4];
#pragma unroll
  for (int tt = 0; tt < 4; ++tt) {
    sa[tt] = ((((panel * 8 + wt * 4 + tt) * 2 + part) * 2 + kh) * 14 + 4 * j + ND * wd) * 1024 + ((((g & 1) << 1) | (g >> 1)) * 16 + r16) * 16;
    asm volatile("" : "+v"(sa[tt]));
  }
  f32x4 acc[NX][4];
#pragma unroll
  for (int dd = 0; dd < NX; ++dd)
#pragma unroll
    for (int tt = 0; tt < 4; ++tt) acc[dd][tt] = f32x4{0.f, 0.f, 0.f, 0.f};
  auto mm = [&](const Frags<ND>& fr, int d0, int d1) {
#pragma unroll
    for (int dd = d0; dd < d1; ++dd)
#pragma unroll
      for (int tt = 0; tt < 4; ++tt) acc[dd][tt] = mfma16(fr.x[dd], fr.w[tt], acc[dd][tt]);
  };
  auto pat = [&](bool tail) {
    if constexpr (ND == 2) {
#pragma unroll
      for (int i = 0; i < 12; ++i) { SGB(0x8, 1); SGB(0x100, 1); if (!tail && i >= 3) SGB(0x2, 1); }
    } else {
#pragma unroll
      for (int i = 0; i < 4; ++i) { SGB(0x8, 1); SGB(0x100, 2); if (!tail) SGB(0x2, 2); }
    }
    if (tail) SGB(0x8, 4);
    SB();
  };
  Frags<ND> FA, FB;
  int w0 = wo0 + SLOT, w1 = wo1 + SLOT, xa[NX];
#pragma unroll
  for (int dd = 0; dd < NX; ++dd) xa[dd] = xo[dd] + SLOT;
#pragma unroll
  for (int tt = 0; tt < 4; ++tt) asm volatile("" : "+v"(acc[NX - 1][tt]));
  BAR();
  loadFrags<ND>(FA, L, wo0, xo, 0);
  SB();
  loadFrags<ND>(FB, L, wo1, xo, 1);
  mm(FA, 0, NX - 1);
  pat(false);
#pragma unroll 1
  for (int s = 2; s <= 16; ++s) {
    mm(FA, NX - 1, NX);
    SB();
    LBAR();
    SB();
    loadFrags<ND>(FA, L, w0, xa, 0);
    mm(FB, 0, NX);
    pat(true);
    loadFrags<ND>(FB, L, w1, xa, 1);
    mm(FA, 0, NX - 1);
    const int so = (s & 3) * SLOT;
    w0 = wo0 | so; w1 = wo1 | so;
#pragma unroll
    for (int dd = 0; dd < NX; ++dd) xa[dd] = xo[dd] | so;
    pat(false);
  }
  mm(FA, NX - 1, NX);
  SB();
#pragma unroll
  for (int c = 0; c < ND; ++c)
#pragma unroll
    for (int tt = 0; tt < 4; ++tt) {
      acc[2 * c][tt] = mfma16(FB.x[2 * c], FB.w[tt], acc[2 * c][tt]);
      acc[2 * c + 1][tt] = mfma16(FB.x[2 * c + 1], FB.w[tt], acc[2 * c + 1][tt]);
    }
#pragma unroll
  for (int c = 0; c < ND; ++c)
#pragma unroll
    for (int tt = 0; tt < 4; ++tt) {
      const f32x4 a = acc[2 * c][tt], b = acc[2 * c + 1][tt];
      const auto s0 = __builtin_amdgcn_permlane16_swap(pk2(a[0], a[1]), pk2(b[0], b[1]), false, false);
      const auto s1 = __builtin_amdgcn_permlane16_swap(pk2(a[2], a[3]), pk2(b[2], b[3]), false, false);
      __builtin_amdgcn_raw_buffer_store_b128(v4u{s0[0], s1[0], s0[1], s1[1]}, rs, sa[tt] + c * 1024, 0, AUX_SC1);
    }
}

DI void loader(LAS unsigned char* L, int lw, int lane, const u16* sre, const u16* sim, int part, const u16* x, const u16* dre, const u16* dim,
               const void* ws, const void* y, int b, int panel, int kh, int j, int mi) {
  constexpr int RAMP = 5;
  const int sz = ((lane & 7) ^ ((4 * (lw & 1) + (lane >> 4)) & 7)) << 4;
  int vw = (lane >> 3) * 4096 + sz;
  int sw0 = (panel * 128 + 8 * lw) * 4096 + kh * 2048;
  int rot = 2 * mi, lo1 = lw * 1024;
  int swa[4];
#pragma unroll
  for (int q = 0; q < 4; ++q) swa[q] = sw0 + (rot & 15) * 128 + q * 32 * 4096;
  int vx[4];
#pragma unroll
  for (int q = 0; q < 4; ++q) {
    const int xc = (lane & 15) ^ (((lane >> 4) | ((q >> 1) << 2)) << 1);
    vx[q] = (4 * q + (lane >> 4)) * 896 + (xc << 4) - q * 1024;
    if (j == 3 && xc >= 8) vx[q] = OOB;
  }
  int sx0 = (kh * 1024 + 16 * lw) * 896 + j * 256, lo4 = lw * 4096 + 16384;
  int sxa = sx0 + (rot & 15) * 64 * 896;
  asm volatile("" : "+v"(vx[0]), "+v"(vx[1]), "+v"(vx[2]), "+v"(vx[3]));
  asm volatile("" : "+s"(sx0), "+s"(lo4), "+s"(sxa));
  asm volatile("" : "+v"(vw));
  asm volatile("" : "+s"(sw0), "+s"(rot), "+s"(lo1), "+s"(swa[0]), "+s"(swa[1]), "+s"(swa[2]), "+s"(swa[3]));
  SB();
  const u16* W = part ? sim : sre;
  const __amdgpu_buffer_rsrc_t rw = __builtin_amdgcn_make_buffer_rsrc((void*)W, 0, SEQ * SEQ * 2, RSRC_W3);
  sfor<0, 4>([&](auto q) { dma16<0>(rw, L + lo1 + q * 4096, vw, swa[q]); });
  SB();
  const __amdgpu_buffer_rsrc_t rx = __builtin_amdgcn_make_buffer_rsrc((void*)x, 0, SEQ * DH * 2, RSRC_W3);
  sfor<0, 4>([&](auto q) { dma16<q * 1024>(rx, L + lo4, vx[q], sxa); });
  SB();
  {
    const int kst = (1 + (j >> 1) + rot) & 15;
    const int sw = sw0 + kst * 128, sx = sx0 + kst * 64 * 896;
    sfor<0, 4>([&](auto q) { dma16<0>(rw, L + SLOT + lo1 + q * 4096, vw, sw + q * 32 * 4096); });
    sfor<0, 4>([&](auto q) { dma16<q * 1024>(rx, L + SLOT + lo4, vx[q], sx); });
  }
  SB();
  u64 otab = j == 0 ? 0xfedcba98765432ull : j == 1 ? 0xbfedc7a9836542ull : j == 2 ? 0xcbfed87a943651ull : 0xdcbfe987a54361ull;
  const u16* Wd = (lw >> 1) ? dim : dre;
  int vd[7];
  {
    const int v0 = (lane >> 3) * 896 + sz + ((lw >> 1) ? 6 * 14336 : 0);
    const int dv = (lw >> 1) ? -14336 : 14336;
#pragma unroll
    for (int i = 0; i < 7; ++i) vd[i] = v0 + i * dv - (i & 1) * 2048;
  }
  int sd0 = (j * 112 + 8 * (lw & 1)) * 896, lod = (lw >> 1) * 14336 + (lw & 1) * 1024;
  asm volatile("" : "+v"(vd[0]), "+v"(vd[1]), "+v"(vd[2]), "+v"(vd[3]), "+v"(vd[4]), "+v"(vd[5]), "+v"(vd[6]));
  asm volatile("" : "+s"(otab), "+s"(Wd), "+s"(sd0), "+s"(lod));
  const __amdgpu_buffer_rsrc_t rd = __builtin_amdgcn_make_buffer_rsrc((void*)Wd, 0, DH * DH * 2, RSRC_W3);
  const __amdgpu_buffer_rsrc_t rs = __builtin_amdgcn_make_buffer_rsrc((void*)ws, 0, WS_BYTES, RSRC_W3);
  if (j == 3) {
    const int pc = ((b & 7) * 8 + mi) * 2 + (lw & 1);
    dma4(rd, L + SPARE + 256 + lw * 256, pc * 4096 + lane * 64);
    const void* tp = (lw & 1) ? y : ws;
    const int tsz = (lw & 1) ? SEQ * DH * 2 : WS_BYTES;
    const int to = (lw & 1) ? (panel * 128 + (kh * 2 + part) * 32 + (lw >> 1) * 31) * 896
                            : (lane & 1) ? EPOCH_OFF + panel * 256 : panel * 458752 + (lw >> 1) * 458624;
    dma4(__builtin_amdgcn_make_buffer_rsrc((void*)tp, 0, tsz, RSRC_W3), L + SPARE + 1280 + lw * 256, to);
  }
  {
    int sw[4], sx, m1, m4, sxc, m4c;
    auto next = [&](int so) {
      const int kst = ((int)otab + rot) & 15;
      otab >>= 4;
#pragma unroll
      for (int q = 0; q < 4; ++q) sw[q] = sw0 + kst * 128 + q * 32 * 4096;
      sx = sx0 + kst * 64 * 896;
      m1 = so + lo1; m4 = so + lo4;
      asm volatile("" : "+s"(sw[0]), "+s"(sw[1]), "+s"(sw[2]), "+s"(sw[3]), "+s"(sx), "+s"(m1), "+s"(m4));
      SB();
    };
    auto Wp = [&]() { sfor<0, 4>([&](auto q) { dma16<0>(rw, L + m1 + q * 4096, vw, sw[q]); }); sxc = sx; m4c = m4; SB(); };
    auto Xp = [&]() { sfor<0, 4>([&](auto q) { dma16<q * 1024>(rx, L + m4c, vx[q], sxc); }); SB(); };
    next(2 * SLOT);
    sfor<2, RAMP + 2>([&](auto nc) {
      constexpr int n = nc;
      if constexpr (n <= RAMP) vbar<8>();
      Wp();
      next(((n + 1) & 3) * SLOT);
      if constexpr (n < RAMP + 1) Xp();
    });
    if (lw == 0) dma4(rs, L + SPARE, lane < 2 ? EPOCH_OFF + panel * 256 + lane * 4 : OOB);
    int so = (RAMP + 3) * SLOT;
#pragma unroll 1
    for (int n = RAMP + 2; n < 16; ++n) {
      Xp();
      vbar<16>();
      Wp();
      next(so & (3 * SLOT));
      so += SLOT;
    }
    Xp();
    vbar<16>();
  }
  SB();
  LAS unsigned char* D = L + lod;
  sfor<0, 49>([&](auto kc) {
    constexpr int k = kc, t = k / 7, i = k % 7;
    constexpr int slot[7] = {XSLOT, 0, SLOT, 2 * SLOT, 3 * SLOT, XSLOT, 0};
    if constexpr (k == 7) vbar<15>();
    if constexpr (k == 14) { vbar<14>(); vbar<0>(); }
    if constexpr (k == 21) { BAR(); }
    if constexpr (k == 28) BAR();
    if constexpr (k == 35 || k == 42) vbar<14>();
    dma16<(i & 1) * 2048>(rd, D + slot[t] + (i >> 1) * 4096, vd[i], sd0 + (t + KROT) % 7 * 128);
  });
  vbar<14>(); vbar<7>();
  WAITV0();
}

__global__ __launch_bounds__(512, 2) __attribute__((aligned(4096)))
void fnet(const u16* __restrict__ x, const u16* __restrict__ sre, const u16* __restrict__ sim,
          const u16* __restrict__ dre, const u16* __restrict__ dim, void* ws, u16* __restrict__ y) {
  __shared__ __attribute__((aligned(16))) unsigned char smem[LDS_BYTES];
  LAS unsigned char* L = (LAS unsigned char*)smem;
  const int lane = threadIdx.x & 63, w = __builtin_amdgcn_readfirstlane(threadIdx.x >> 6);
  const int b = blockIdx.x, xcd = b & 7, r = b >> 3;
  const int kh = xcd & 1, mi = r >> 2, j = r & 3;
  const int panel = (xcd >> 1) * 4 + (mi >> 1), part = mi & 1;
  const int mb = kh * 2 + part, nb = j;
  HOP(w, pad2, pad1, pad0);
  if (__builtin_expect(w >= 4, 1)) {
    const int lw = w - 4;
    loader(L, lw, lane, sre, sim, part, x, dre, dim, ws, y, b, panel, kh, j, mi);
    BAR();
    BAR();
    asm volatile("s_endpgm\n\t.p2align 12" ::: "memory");
    __builtin_unreachable();
  } else {
    const int cw = w;
    const __amdgpu_buffer_rsrc_t rs = __builtin_amdgcn_make_buffer_rsrc(ws, 0, WS_BYTES, RSRC_W3);
    const int pb = cw >> 1, ti = cw & 1;
    const int g = lane >> 4, e = lane & 15;
    int wa[3][2];
#pragma unroll
    for (int i = 0; i < 3; ++i) {
      wa[i][0] = i * 65536 + pb * 14336 + e * 128 + ((g ^ (e >> 1)) << 4);
      wa[i][1] = wa[i][0] ^ 64;
      asm volatile("" : "+v"(wa[i][0]), "+v"(wa[i][1]));
    }
    int vo = FLAG_OFF + ((panel * 4 + mb) * 4 + nb) * 256 + pb * 128 + (lane < 32 ? lane * 4 : OOB);
    int fo = FLAG_OFF + (panel * 16 + (cw & 1) * 8 + lane) * 256 + part * 128 + (kh * 8 + 2 * j + (cw >> 1)) * 8;
    fo = lane < 8 ? fo : OOB;
    int pv = lane * 16;
    int pqb = (((panel * 8 + mb * 2 + ti) * 2 + pb) * 2) * 14 * 1024;
    int xw = cw * SLOT + 28672 + lane * 16, xr = xw ^ (2 * SLOT);
    int ya = ((panel * 128 + mb * 32 + ti * 16 + e) * DH + nb * 112) * 2 + 16 * (g >> 1) + (pb ? 192 - 32 * (g & 1) : 32 * (g & 1));
    int yb = pb ? ((g & 1) ? OOB : ya - 64) : ya + 64;
    unsigned sgn = pb ? 0x80008000u : 0u;
    asm volatile("" : "+v"(vo), "+v"(fo), "+v"(pv), "+v"(xw), "+v"(xr), "+v"(ya), "+v"(yb));
    asm volatile("" : "+s"(sgn));
    asm volatile("" : "+s"(pqb));
    const __amdgpu_buffer_rsrc_t ry = __builtin_amdgcn_make_buffer_rsrc((void*)y, 0, SEQ * DH * 2, RSRC_W3);
    if (j < 3) phaseA_compute<2>(L, cw, lane, rs, panel, part, kh, j);
    else phaseA_compute<1>(L, cw, lane, rs, panel, part, kh, j);
    PADA(2, ret);
    const v2u ep = *(LAS v2u*)(L + SPARE);
    LBAR();
    bf16x8 wf[14][7];
    auto ldw = [&](int i) {
      constexpr int slot[7] = {XSLOT, 0, SLOT, 2 * SLOT, 3 * SLOT, XSLOT, 0};
      const int so = slot[i >> 1];
#pragma unroll
      for (int q = 0; q < 7; ++q) wf[i][q] = *(LAS bf16x8*)(L + wa[so >> 16][i & 1] + (so & 65535) + q * 2048);
    };
#pragma unroll
    for (int i = 0; i < 4; ++i) ldw(i);
    const u64 tag = ((u64)ep.y << 32 | ep.x) + 1;
    v2u tg = v2u{(unsigned)tag, (unsigned)(tag >> 32)};
    int tagv = (lane & 1) ? tg.y : tg.x;
    asm volatile("" : "+v"(tg), "+v"(tagv));
    WAITV0();
    __builtin_amdgcn_raw_buffer_store_b64(tg, rs, fo, 0, AUX_SC1);
    LBAR();
    {
      unsigned spins = 0, have = 0;
      while (have != ~0u) {
        const int v = __builtin_amdgcn_raw_ptr_buffer_atomic_add_i32(0, rs, vo, 0, AUX_SC1);
        have = (unsigned)__ballot(v == tagv);
        if (++spins > (1u << 16)) have = ~0u;
      }
    }
    constexpr int PD = 4;
    v4u pq[14][2];
    auto ldpq = [&](int i) {
      const int kap = (i + 2 * KROT) % 14;
#pragma unroll
      for (int h2 = 0; h2 < 2; ++h2) pq[i][h2] = __builtin_amdgcn_raw_buffer_load_b128(rs, pv, pqb + (h2 * 14 + kap) * 1024, AUX_SC1);
    };
#pragma unroll
    for (int i = 0; i < PD; ++i) ldpq(i);
    SB();
    BAR();
    SB();
    f32x4 acc[7];
#pragma unroll
    for (int q = 0; q < 7; ++q) acc[q] = f32x4{0, 0, 0, 0};
#pragma unroll
    for (int i = 0; i < 13; ++i) {
      if (i + PD < 14) ldpq(i + PD);
      if (i >= 3) ldw(i + 1);
#pragma unroll
      for (int h2 = 0; h2 < 2; ++h2)
#pragma unroll
        for (int q = 0; q < 7; ++q) acc[q] = mfma16(wf[i][q], __builtin_bit_cast(bf16x8, pq[i][h2]), acc[q]);
#pragma unroll
      for (int q = 0; q < 7; ++q) { SGB(0x8, 1); SGB(0x100, 1); }
      SGB(0x8, 1); SGB(0x4, 3); SGB(0x8, 1); SGB(0x4, 3);
      SGB(0x8, 1); SGB(0x20, 1); SGB(0x8, 2); SGB(0x20, 1); SGB(0x8, 2);
      SB();
      if (i >= 2 && i <= 10 && !(i & 1)) { BAR(); SB(); }
      if (i == 6) { PAD(3, ret); SB(); }
      if (i == 10) { PAD(4, ret); SB(); }
      if (i == 2) { PAD(1, pad4); SB(); }
    }
#pragma unroll
    for (int h2 = 0; h2 < 2; ++h2)
#pragma unroll
      for (int q = 3; q < 7; ++q) acc[q] = mfma16(wf[13][q], __builtin_bit_cast(bf16x8, pq[13][h2]), acc[q]);
#pragma unroll
    for (int q = 0; q < 3; ++q) acc[q] = mfma16(wf[13][q], __builtin_bit_cast(bf16x8, pq[13][0]), acc[q]);
    SB();
#pragma unroll
    for (int q = 3; q < 7; ++q) *(LAS f32x4*)(L + xw + (q - 3) * 1024) = acc[q];
    SB();
#pragma unroll
    for (int q = 0; q < 3; ++q) acc[q] = mfma16(wf[13][q], __builtin_bit_cast(bf16x8, pq[13][1]), acc[q]);
    SB();
    LBAR();
    f32x4 o[4];
#pragma unroll
    for (int q = 0; q < 4; ++q) o[q] = *(LAS f32x4*)(L + xr + (3 - q) * 1024);
#pragma unroll
    for (int p2 = 0; p2 < 2; ++p2) {
      const f32x4 a = acc[2 * p2] - o[2 * p2], c = acc[2 * p2 + 1] - o[2 * p2 + 1];
      const auto s0 = __builtin_amdgcn_permlane16_swap(pk2(a[0], a[1]) ^ sgn, pk2(c[0], c[1]) ^ sgn, false, false);
      const auto s1 = __builtin_amdgcn_permlane16_swap(pk2(a[2], a[3]) ^ sgn, pk2(c[2], c[3]) ^ sgn, false, false);
      __builtin_amdgcn_raw_buffer_store_b128(v4u{s0[0], s1[0], s0[1], s1[1]}, ry, p2 ? yb : ya, 0, 0);
    }
    SB();
    if (threadIdx.x == 0 && mb == 0 && nb == 0)
      __builtin_amdgcn_raw_buffer_store_b64(tg, rs, EPOCH_OFF + panel * 256, 0, AUX_SC1);
  }
}

extern "C" void kernel_launch(void* const* d_in, const int* in_sizes, int n_in, void* d_out, int out_size, void* d_ws, size_t ws_size,
                              hipStream_t stream) {
  fnet<<<256, 512, 0, stream>>>((const u16*)d_in[0], (const u16*)d_in[1], (const u16*)d_in[2], (const u16*)d_in[3], (const u16*)d_in[4], d_ws, (u16*)d_out);
}
```
